```python
import torch

def _softmax(x, axis=-1):
    return torch.softmax(x, dim=axis)
def _gelu(x):
    return torch.nn.functional.gelu(x, approximate="tanh")
def _silu(x):
    return torch.nn.functional.silu(x)
def _sigmoid(x):
    return torch.sigmoid(x)
def _layernorm(x, axis=-1, eps=1e-5):
    m = x.mean(dim=axis, keepdim=True)
    v = x.var(dim=axis, keepdim=True, unbiased=False)
    return (x - m) / torch.sqrt(v + eps)
def _scatter_add(tgt, idx, src, axis=0):
    return tgt.clone().index_add_(axis, idx, src)
def _segment_sum(src, seg, n):
    out = torch.zeros((n,) + src.shape[1:], dtype=src.dtype)
    return out.index_add_(0, seg, src)
def _pad(x, pw):
    flat = tuple(v for pair in reversed(pw) for v in pair)
    return torch.nn.functional.pad(x, flat)

kernel_name = 'sequential_preLN__sliding_block_cached__dense_activation_relu__decode'

def setup_inputs():

    _w = (lambda a: (lambda t: t.to(torch.bfloat16) if t.is_floating_point() else t)(torch.as_tensor(a)))
    def _A_setup():
        B, Sq, D = 1, 1, 640
        Hq, Hkv, Sk, Dh = 32, 32, 4096, 128
        assert Hq % Hkv == 0, (Hq, Hkv)
        return {
            "x": _w((lambda *s: torch.randn(*s, dtype=torch.bfloat16) * 0.05)(B, Sq, D)),
            "k_cache": _w((lambda *s: torch.randn(*s, dtype=torch.bfloat16) * 3.2)(B, Hkv, Sk, Dh)),
            "v_cache": _w((lambda *s: torch.randn(*s, dtype=torch.bfloat16) * 0.4)(B, Hkv, Sk, Dh)),
            "wq": _w((lambda *s: torch.randn(*s, dtype=torch.bfloat16) * 3.2)(D, Hq * Dh) * (D ** -0.5)),
            "wo": _w((lambda *s: torch.randn(*s, dtype=torch.bfloat16) * 0.4)(Hq * Dh, D) * ((Hq * Dh) ** -0.5)),
        }
    _A_in = _A_setup()

    def _B_setup():
        B, S, D, Dff = 1, 1, 640, 1728
        return {
            "x": _w((lambda *s: torch.randn(*s, dtype=torch.bfloat16) * 0.05)(B, S, D)),
            "w1": _w((lambda *s: torch.randn(*s, dtype=torch.bfloat16) * 0.05)(D, Dff)),
            "w2": _w((lambda *s: torch.randn(*s, dtype=torch.bfloat16) * 0.05)(Dff, D)),
        }
    _B_in = _B_setup()

    return {
        "A_x": _A_in["x"],
        "A_k_cache": _A_in["k_cache"],
        "A_v_cache": _A_in["v_cache"],
        "A_wq": _A_in["wq"],
        "A_wo": _A_in["wo"],
        "B_x": _B_in["x"],
        "B_w1": _B_in["w1"],
        "B_w2": _B_in["w2"],
    }

def reference(A_x, A_k_cache, A_v_cache, A_wq, A_wo, B_x, B_w1, B_w2):
    def _A_ref(x, k_cache, v_cache, wq, wo):
        B, Sq, D = x.shape
        Hq, Dh = 32, 128
        Hkv, Sk = k_cache.shape[1], k_cache.shape[2]
        W = min(4096, max(256, Sk // 4))
        rep = Hq // Hkv
        q = (lambda x, *d: x.permute(*d))((x @ wq).reshape(B, Sq, Hq, Dh), 0, 2, 1, 3)
        k = (lambda x, n, axis: torch.repeat_interleave(x, n, dim=axis))(k_cache[:, :, Sk - W:, :], rep, axis=1)
        v = (lambda x, n, axis: torch.repeat_interleave(x, n, dim=axis))(v_cache[:, :, Sk - W:, :], rep, axis=1)
        scores = torch.einsum("bhqd,bhkd->bhqk", q, k) * (Dh ** -0.5)
        p = _softmax(scores, axis=-1)
        o = torch.einsum("bhqk,bhkd->bhqd", p, v)
        return (lambda x, *d: x.permute(*d))(o, 0, 2, 1, 3).reshape(B, Sq, Hq * Dh) @ wo

    def _B_ref(x, w1, w2):
        h = torch.einsum("bsd,df->bsf", x, w1)
        zero = torch.zeros_like(h)
        h = torch.maximum(h, zero)
        return torch.einsum("bsf,fd->bsd", h, w2)

    x = A_x
    h = x + _A_ref(_layernorm(x, axis=-1), A_k_cache, A_v_cache, A_wq, A_wo)
    h2 = h + _B_ref(_layernorm(h, axis=-1), B_w1, B_w2)
    return h2
```

```cpp
#include <hip/hip_runtime.h>
#include <hip/hip_bf16.h>
typedef unsigned short u16;
__device__ __forceinline__ float bf2f(u16 b){ return __uint_as_float(((unsigned)b)<<16);}
__device__ __forceinline__ u16 f2bf(float f){ unsigned u=__float_as_uint(f); u += 0x7fffu + ((u>>16)&1u); return (u16)(u>>16);}
__device__ __forceinline__ float rbf(float f){ return bf2f(f2bf(f)); }
constexpr int D=640, H=32, DH=128, SK=4096, W=1024, DFF=1728;

__device__ void layernorm_block(const float* xin, float* xn, float* red){
  int t=threadIdx.x; float s=0; for(int i=t;i<D;i+=blockDim.x) s+=xin[i];
  for(int o=32;o>0;o>>=1) s+=__shfl_xor(s,o); if((t&63)==0) red[t>>6]=s; __syncthreads();
  float m=0; for(int i=0;i<(int)blockDim.x/64;i++) m+=red[i]; m/=D; __syncthreads();
  float v=0; for(int i=t;i<D;i+=blockDim.x){ float d=xin[i]-m; v+=d*d; }
  for(int o=32;o>0;o>>=1) v+=__shfl_xor(v,o); if((t&63)==0) red[t>>6]=v; __syncthreads();
  float vv=0; for(int i=0;i<(int)blockDim.x/64;i++) vv+=red[i]; vv/=D; __syncthreads();
  float r=1.0f/sqrtf(vv+1e-5f);
  for(int i=t;i<D;i+=blockDim.x) xn[i]=rbf((xin[i]-m)*r);
  __syncthreads();
}
__global__ void k_q(const u16* x,const u16* wq,float* q){
  __shared__ float xs[D], xn[D], red[8];
  for(int i=threadIdx.x;i<D;i+=blockDim.x) xs[i]=bf2f(x[i]); __syncthreads();
  layernorm_block(xs,xn,red);
  int n=blockIdx.x*blockDim.x+threadIdx.x; float a=0; for(int k=0;k<D;k++) a+=xn[k]*bf2f(wq[(size_t)k*4096+n]); q[n]=rbf(a);
}
__global__ void k_attn(const float* q,const u16* K,const u16* V,float* o){
  __shared__ float s[W], red[8], qs[DH]; int h=blockIdx.x,t=threadIdx.x;
  if(t<DH) qs[t]=q[h*DH+t]; __syncthreads();
  const u16* Kh=K+((size_t)h*SK+(SK-W))*DH; const u16* Vh=V+((size_t)h*SK+(SK-W))*DH;
  float m=-INFINITY;
  for(int j=t;j<W;j+=blockDim.x){ float a=0; for(int d=0;d<DH;d++) a+=qs[d]*bf2f(Kh[(size_t)j*DH+d]); a*=0.08838834764831845f; s[j]=a; m=fmaxf(m,a);}
  for(int o2=32;o2>0;o2>>=1) m=fmaxf(m,__shfl_xor(m,o2)); if((t&63)==0) red[t>>6]=m; __syncthreads();
  m=red[0]; for(int i=1;i<(int)blockDim.x/64;i++) m=fmaxf(m,red[i]); __syncthreads();
  float l=0; for(int j=t;j<W;j+=blockDim.x){ float p=__expf(s[j]-m); s[j]=p; l+=p; }
  for(int o2=32;o2>0;o2>>=1) l+=__shfl_xor(l,o2); if((t&63)==0) red[t>>6]=l; __syncthreads();
  l=0; for(int i=0;i<(int)blockDim.x/64;i++) l+=red[i];
  if(t<DH){ float a=0; for(int j=0;j<W;j++) a+=s[j]*bf2f(Vh[(size_t)j*DH+t]); o[h*DH+t]=rbf(a/l);}
}
__global__ void k_wo(const float* o,const u16* wo,const u16* x,float* h){
  int n=blockIdx.x*blockDim.x+threadIdx.x; float a=0; for(int k=0;k<4096;k++) a+=o[k]*bf2f(wo[(size_t)k*D+n]); h[n]=rbf(bf2f(x[n])+rbf(a));
}
__global__ void k_w1(const float* h,const u16* w1,float* f){
  __shared__ float xs[D], xn[D], red[8];
  for(int i=threadIdx.x;i<D;i+=blockDim.x) xs[i]=h[i]; __syncthreads();
  layernorm_block(xs,xn,red);
  int n=blockIdx.x*blockDim.x+threadIdx.x; float a=0; for(int k=0;k<D;k++) a+=xn[k]*bf2f(w1[(size_t)k*DFF+n]); f[n]=fmaxf(rbf(a),0.f);
}
__global__ void k_w2(const float* f,const u16* w2,const float* h,u16* out){
  int n=blockIdx.x*blockDim.x+threadIdx.x; float a=0; for(int k=0;k<DFF;k++) a+=f[k]*bf2f(w2[(size_t)k*D+n]); out[n]=f2bf(h[n]+rbf(a));
}
extern "C" void kernel_launch(void* const* d_in, const int* in_sizes, int n_in, void* d_out, int out_size, void* d_ws, size_t ws_size, hipStream_t stream) {
  const u16* x=(const u16*)d_in[0]; const u16* K=(const u16*)d_in[1]; const u16* V=(const u16*)d_in[2];
  const u16* wq=(const u16*)d_in[3]; const u16* wo=(const u16*)d_in[4]; const u16* w1=(const u16*)d_in[6]; const u16* w2=(const u16*)d_in[7];
  float* ws=(float*)d_ws; float* q=ws; float* o=ws+4096; float* h=ws+8192; float* f=ws+9216;
  k_q<<<64,64,0,stream>>>(x,wq,q);
  k_attn<<<32,256,0,stream>>>(q,K,V,o);
  k_wo<<<10,64,0,stream>>>(o,wo,x,h);
  k_w1<<<27,64,0,stream>>>(h,w1,f);
  k_w2<<<10,64,0,stream>>>(f,w2,h,(u16*)d_out);
}
```

```cpp
#define PROBE_B 0
#define PROBE_K 0
#define PROBE_ROLE (w==15)
#define PROBE_CYC 0
#include <hip/hip_runtime.h>
#include <hip/hip_bf16.h>

typedef unsigned u32;
typedef unsigned long long u64;
typedef u32 v4u __attribute__((ext_vector_type(4)));
typedef u32 v2u __attribute__((ext_vector_type(2)));
typedef float v4f __attribute__((ext_vector_type(4)));
typedef float v2f __attribute__((ext_vector_type(2)));
typedef __bf16 bf16x2 __attribute__((ext_vector_type(2)));
typedef __amdgpu_buffer_rsrc_t rsrc_t;
#define LAS __attribute__((address_space(3)))
#define GAS __attribute__((address_space(1)))

#define AUX_KV 2
#define AUX_WQ 2
#define AUX_WO 2
#define AUX_W1 2
#define AUX_W2 2
#define DLY_Q 12
#define DLY_A 8
#define DLY_M 80
#define V_EARLY 2

#define CODE_BYTES 12288
constexpr int SC1 = 16;
constexpr u32 SENT = 0xFFFFFFFFu;
constexpr int OOB = 0x40000000;
constexpr int SPIN = 1 << 15;
constexpr int WS_E = 0, WS_Q = 0x10000, WS_SIG = 0x40000, WS_GR = 0x80000, GR_SZ = 0x20000;
constexpr int GR_AO = 0, GR_AML = 0x1800, GR_B = 0x4000, GR_D = 0x10000;
constexpr int WS_BYTES = WS_GR + 8 * GR_SZ;
constexpr int WS_TS = 0x800000;
constexpr int L_FLAG = 0;
constexpr int F_XN = 0, F_QCNT = 1, F_VGO = 2, F_QRDY = 3, F_MCNT = 4, F_OCNT = 5, F_YCNT = 6, F_VCNT = 7, F_APUB = 8, F_E0 = 10, F_E1 = 11;
constexpr int L_M = 64;
constexpr int L_L = 96;
constexpr int L_Q = 128;
constexpr int L_XN = 512;
constexpr int L_QP = 512 + 2560;
constexpr int L_OP = L_QP + 4096;
constexpr int L_YP = L_OP + 4096;
constexpr int L_END = L_YP + 1024;
constexpr int L_H = 512;
constexpr int L_ST = 64;
constexpr int L_P1 = L_QP;

#define TS(i) do { if ((i) == 0 || (i) == PROBE_K) { u64 t_; asm volatile("s_memrealtime %0\n\ts_waitcnt lgkmcnt(0)" : "=s"(t_) :: "memory"); \
  if ((i) == 0) { pt0 = (u32)t_; asm volatile("s_memtime %0\n\ts_waitcnt lgkmcnt(0)" : "=s"(pc0) :: "memory"); } else pt1 = (u32)t_; } } while (0)
#define RET() do { if (b == PROBE_B && (PROBE_ROLE) && (PROBE_K == 0 || pt1 != 0)) { \
  if (PROBE_K == 0 && PROBE_CYC) { for (;;) { u64 t_; asm volatile("s_memtime %0\n\ts_waitcnt lgkmcnt(0)" : "=s"(t_) :: "memory"); if (t_ - pc0 >= PROBE_CYC) break; } } \
  else { const u32 dt = PROBE_K ? pt1 - pt0 : 0; for (;;) { u64 t_; asm volatile("s_memrealtime %0\n\ts_waitcnt lgkmcnt(0)" : "=s"(t_) :: "memory"); if ((u32)t_ - pt0 >= 6000 + 20 * dt) break; } } } return; } while (0)

struct Args { const char* x; const char* K; const char* V; const char* wq; const char* wo; const char* w1; const char* w2; char* out; char* ws; };

__device__ __forceinline__ rsrc_t mk(const char* p, int n) { return __builtin_amdgcn_make_buffer_rsrc((void*)p, 0, n, 0x00020000); }
template <int AUX> __device__ __forceinline__ v4u ld16(rsrc_t r, int off) { return __builtin_amdgcn_raw_buffer_load_b128(r, off, 0, AUX); }
__device__ __forceinline__ float asf(u32 x) { return __builtin_bit_cast(float, x); }
__device__ __forceinline__ u32 asu(float x) { return __builtin_bit_cast(u32, x); }
__device__ __forceinline__ float blo(u32 x) { return asf(x << 16); }
__device__ __forceinline__ float bhi(u32 x) { return asf(x & 0xffff0000u); }
__device__ __forceinline__ u32 pk(float lo, float hi) { bf16x2 v = {(__bf16)lo, (__bf16)hi}; return __builtin_bit_cast(u32, v); }
__device__ __forceinline__ float rbf(float x) { return blo(pk(x, 0.f) & 0xffffu); }
__device__ __forceinline__ float dot2(u32 a, u32 b, float c) { return __builtin_amdgcn_fdot2_f32_bf16(__builtin_bit_cast(bf16x2, a), __builtin_bit_cast(bf16x2, b), c, false); }
template <int C> __device__ __forceinline__ float dpp(float x) { return asf(__builtin_amdgcn_mov_dpp(asu(x), C, 0xf, 0xf, true)); }
constexpr int X1 = 0xB1, X2 = 0x4E, HM = 0x141, R4 = 0x124, R8 = 0x128;
__device__ __forceinline__ float sw32(float a, float b) { auto r = __builtin_amdgcn_permlane32_swap(asu(a), asu(b), false, false); return asf(r[0]) + asf(r[1]); }
__device__ __forceinline__ float sw16(float a, float b) { auto r = __builtin_amdgcn_permlane16_swap(asu(a), asu(b), false, false); return asf(r[0]) + asf(r[1]); }
__device__ __forceinline__ float mx32(float a) { auto r = __builtin_amdgcn_permlane32_swap(asu(a), asu(a), false, false); return fmaxf(asf(r[0]), asf(r[1])); }
__device__ __forceinline__ float mx16(float a) { auto r = __builtin_amdgcn_permlane16_swap(asu(a), asu(a), false, false); return fmaxf(asf(r[0]), asf(r[1])); }
__device__ __forceinline__ float rowsum(float x) { x += dpp<X1>(x); x += dpp<X2>(x); x += dpp<HM>(x); x += dpp<R8>(x); return x; }
__device__ __forceinline__ float wavesum(float x) { x = rowsum(x); x = sw16(x, x); return sw32(x, x); }
__device__ __forceinline__ void unpack8(v4u v, float (&f)[8]) {
  f[0] = blo(v.x); f[1] = bhi(v.x); f[2] = blo(v.y); f[3] = bhi(v.y); f[4] = blo(v.z); f[5] = bhi(v.z); f[6] = blo(v.w); f[7] = bhi(v.w);
}
__device__ __forceinline__ bool valid4(v4u v) { return (v.x != SENT) & (v.y != SENT) & (v.z != SENT) & (v.w != SENT); }
__device__ __forceinline__ u32 nosent(u32 x) { return x < 0xFFFFFFFEu ? x : 0xFFFFFFFEu; }
#define FENCE() asm volatile("" ::: "memory")
#define SB() __builtin_amdgcn_sched_barrier(0)
template <int N> __device__ __forceinline__ void nap() {
#pragma unroll
  for (int i = 0; i < N / 100; ++i) __builtin_amdgcn_s_sleep(100);
  if (N % 100) __builtin_amdgcn_s_sleep(N % 100);
}
__device__ __forceinline__ void lds_wait(LAS u32* f, u32 want) {
  _Pragma("unroll 1") for (int i = 0; i < (SPIN << 3); ++i) {
    if (__builtin_amdgcn_readfirstlane(__hip_atomic_load(f, __ATOMIC_RELAXED, __HIP_MEMORY_SCOPE_WORKGROUP)) >= want) break;
    __builtin_amdgcn_s_sleep(1);
  }
  FENCE();
}
__device__ __forceinline__ void lds_set(LAS u32* f) { FENCE(); __hip_atomic_store(f, 1u, __ATOMIC_RELAXED, __HIP_MEMORY_SCOPE_WORKGROUP); FENCE(); }
__device__ __forceinline__ u32 lds_arrive(LAS u32* f, int l) {
  FENCE();
  u32 old = 0;
  if (l == 0) old = __hip_atomic_fetch_add(f, 1u, __ATOMIC_RELAXED, __HIP_MEMORY_SCOPE_WORKGROUP);
  old = __builtin_amdgcn_readfirstlane(old);
  FENCE();
  return old;
}
__device__ __forceinline__ void ln_consts(float s1, float s2, float& mb, float& rinv) {
  const float mean = s1 * (1.f / 640.f);
  const float var = fmaxf(s2 * (1.f / 640.f) - mean * mean, 0.f);
  mb = rbf(mean);
  rinv = 1.f / rbf(__builtin_amdgcn_sqrtf(rbf(rbf(var) + 1e-5f)));
}

__global__ void __launch_bounds__(1024) fused_decode_kernel(Args a) {
  __shared__ __attribute__((aligned(16))) char smem[L_END];
  LAS char* L = (LAS char*)smem;
  LAS u32* F = (LAS u32*)smem;
  const int b = blockIdx.x, l = threadIdx.x & 63, w = __builtin_amdgcn_readfirstlane(threadIdx.x >> 6);
  const int g = b & 7, j = b >> 3;
  u32 tsv = 0, pt0 = 0, pt1 = 0; u64 pc0 = 0; (void)tsv; (void)pt0; (void)pt1; (void)pc0;
  u64 pc; asm volatile("s_getpc_b64 %0" : "=s"(pc));
  TS(0);
  const rsrc_t rW = mk(a.ws, WS_BYTES);
  if (j >= 24 && w >= 14) {
    const rsrc_t rC = mk((const char*)(pc & ~255ull), CODE_BYTES);
    u32 v = __builtin_amdgcn_raw_buffer_load_b32(rC, ((w - 14) * 64 + l) * 128, 0, 0);
    asm volatile("s_waitcnt vmcnt(0)" :: "v"(v) : "memory");
    RET();
  }
  if (__builtin_expect(j < 24, 1)) {
    const int hl = j / 6, s = j - 6 * hl, h = 4 * g + hl;
    const bool isQ = w < 8;
    const int kw = w & 7;
    const int u0 = 43 * s - (s > 4 ? 1 : 0), nu = s < 4 ? 43 : 42;
    const int uq0 = 27 * s - (s > 4 ? 1 : 0), nuq = s < 4 ? 27 : 26;
    const size_t kvoff = ((size_t)h * 4096 + 3072 + 4 * u0) * 256;
    const rsrc_t r0 = mk(isQ ? a.wq + h * 256 + (size_t)uq0 * 32768 : a.K + kvoff, isQ ? (4 * nuq - 1) * 8192 + 256 : nu * 1024);
    const int step = isQ ? 262144 : 8192;
    const int off0 = isQ ? (kw * 4 + (l >> 4)) * 8192 + (l & 15) * 16 : kw * 1024 + l * 16;
    v4u xa = {0, 0, 0, 0}, xb = {0, 0, 0, 0};
    if (w == 7) { const rsrc_t rx = mk(a.x, 1280); xa = ld16<0>(rx, l * 16); xb = ld16<0>(rx, 1024 + l * 16); }
    v4u kq[6];
    static_assert(AUX_WQ == AUX_KV, "shared first loads");
    kq[0] = ld16<AUX_KV>(r0, off0);
    kq[1] = ld16<AUX_KV>(r0, off0 + step);
    SB();
    if (w == 15 && l < 16) F[l] = 0;
    asm volatile("s_waitcnt lgkmcnt(0)" ::: "memory");
    __builtin_amdgcn_s_barrier();
    SB();
    kq[2] = ld16<AUX_KV>(r0, off0 + 2 * step);
    kq[3] = ld16<AUX_KV>(r0, off0 + 3 * step);
    SB();
    const int gr = WS_GR + g * GR_SZ;
    if (isQ) {
      if (w == 6) {
        const v2u e = __builtin_amdgcn_raw_buffer_load_b64(rW, WS_E, 0, SC1);
        const v4u sv = {SENT, SENT, SENT, SENT};
        __builtin_amdgcn_raw_buffer_store_b128(sv, rW, l < 16 ? gr + GR_AO + j * 256 + l * 16 : l < 24 ? gr + GR_AML + j * 128 + (l - 16) * 16 : OOB, 0, SC1);
        __builtin_amdgcn_raw_buffer_store_b128(sv, rW, s < 5 ? WS_GR + (l >> 3) * GR_SZ + GR_B + (g * 20 + hl * 5 + s) * 128 + (l & 7) * 16 : OOB, 0, SC1);
        const u64 t = (((u64)e.y << 32) | e.x) + 1;
        asm volatile("s_waitcnt vmcnt(0)" ::: "memory");
        if (l == 0) { F[F_E0] = (u32)t; F[F_E1] = (u32)(t >> 32); }
      }
      if (w == 7) {
        float f[8], e[8];
        unpack8(xa, f); unpack8(xb, e);
        float s1 = 0.f, s2 = 0.f;
#pragma unroll
        for (int i = 0; i < 8; ++i) { s1 += f[i] + e[i]; s2 = fmaf(f[i], f[i], fmaf(e[i], e[i], s2)); }
        s1 = wavesum(s1); s2 = wavesum(s2);
        float mb, rinv; ln_consts(s1, s2, mb, rinv);
#pragma unroll
        for (int i = 0; i < 8; ++i) { f[i] = rbf(rbf(f[i] - mb) * rinv); e[i] = rbf(rbf(e[i] - mb) * rinv); }
        LAS v4f* xn = (LAS v4f*)(L + L_XN);
        xn[2 * l] = v4f{f[0], f[1], f[2], f[3]}; xn[2 * l + 1] = v4f{f[4], f[5], f[6], f[7]};
        if (l < 16) { xn[128 + 2 * l] = v4f{e[0], e[1], e[2], e[3]}; xn[129 + 2 * l] = v4f{e[4], e[5], e[6], e[7]}; }
        lds_set(F + F_XN);
      }
      lds_wait(F + F_XN, 1);
      float acc[8] = {0, 0, 0, 0, 0, 0, 0, 0};
#pragma unroll
      for (int i = 0; i < 4; ++i) {
        const int row = min(4 * (uq0 + i * 8 + kw) + (l >> 4), 639);
        const float xv = ((LAS float*)(L + L_XN))[row];
        float f[8]; unpack8(kq[i], f);
#pragma unroll
        for (int e = 0; e < 8; ++e) acc[e] = fmaf(xv, f[e], acc[e]);
      }
      {
        const float t0 = sw32(acc[0], acc[4]), t1 = sw32(acc[1], acc[5]), t2 = sw32(acc[2], acc[6]), t3 = sw32(acc[3], acc[7]);
        ((LAS v2f*)(L + L_QP))[kw * 64 + 4 * (l & 15) + (l >> 4)] = v2f{sw16(t0, t2), sw16(t1, t3)};
      }
      TS(1);
      if (lds_arrive(F + F_QCNT, l) == 7) {
        v2f q = {0.f, 0.f};
#pragma unroll
        for (int i = 0; i < 8; ++i) q += ((LAS v2f*)(L + L_QP))[i * 64 + l];
        const u32 T0 = F[F_E0], T1 = F[F_E1] & 0xffu;
        __builtin_amdgcn_raw_buffer_store_b128(v4u{T0, (asu(q.x) & ~0xffu) | T1, T0, (asu(q.y) & ~0xffu) | T1}, rW, WS_Q + (h * 6 + s) * 1024 + l * 16, 0, SC1);
        TS(2);
        nap<DLY_Q>();
        float q0, q1;
        _Pragma("unroll 1") for (int it = 0; it < SPIN; ++it) {
          FENCE();
          v4u v[6];
#pragma unroll
          for (int p = 0; p < 6; ++p) v[p] = ld16<SC1>(rW, WS_Q + (h * 6 + p) * 1024 + l * 16);
          const v4u pr = ld16<SC1>(rW, WS_Q + (g * 24 + min(l, 23)) * 1024);
          SB();
          if (it == 0) lds_set(F + F_VGO);
          SB();
          bool ok = (pr.x == T0) & (pr.z == T0) & ((pr.y & 0xffu) == T1) & ((pr.w & 0xffu) == T1);
          q0 = 0.f; q1 = 0.f;
#pragma unroll
          for (int p = 0; p < 6; ++p) {
            ok &= (v[p].x == T0) & (v[p].z == T0) & ((v[p].y & 0xffu) == T1) & ((v[p].w & 0xffu) == T1);
            q0 += asf(v[p].y & ~0xffu); q1 += asf(v[p].w & ~0xffu);
          }
          if (__all(ok)) break;
        }
        ((LAS u32*)(L + L_Q))[l] = pk(q0, q1);
        lds_set(F + F_QRDY);
        TS(3);
      }
      if (s == 5) RET();
      const int cb = hl * 5 + s;
      const rsrc_t rO = mk(a.wo + (size_t)(g * 512 + kw * 64) * 1280 + cb * 64, 63 * 1280 + 64);
      lds_wait(F + F_VCNT, 8);
      v4u wv[4];
#pragma unroll
      for (int i = 0; i < 4; ++i) wv[i] = ld16<AUX_WO>(rO, (4 * (l >> 2) + i) * 1280 + (l & 3) * 16);
      SB();
      lds_wait(F + F_QRDY, 1);
      lds_wait(F + F_APUB, 1);
      nap<DLY_A>();
      TS(4);
      const int aoff = l < 48 ? gr + GR_AO + ((kw >> 1) * 6 + (l >> 3)) * 256 + (kw & 1) * 128 + (l & 7) * 16
                              : l < 54 ? gr + GR_AML + ((kw >> 1) * 6 + l - 48) * 128 : OOB;
      v4u av;
      _Pragma("unroll 1") for (int it = 0; it < SPIN; ++it) {
        FENCE();
        av = ld16<SC1>(rW, aoff);
        if (__all(valid4(av))) break;
      }
      TS(5);
      float fo;
      {
        const bool ml = l >= 48 && l < 54;
        const float m = ml ? asf(av.x) : -INFINITY, ls = ml ? asf(av.y) : 0.f;
        float M = fmaxf(m, dpp<X1>(m)); M = fmaxf(M, dpp<X2>(M)); M = fmaxf(M, dpp<HM>(M));
        const float al = __builtin_amdgcn_exp2f(m - M);
        float Ls = al * ls; Ls += dpp<X1>(Ls); Ls += dpp<X2>(Ls); Ls += dpp<HM>(Ls);
        const float fp = ml ? al * __builtin_amdgcn_rcpf(Ls) : 0.f;
        fo = asf(__builtin_amdgcn_ds_bpermute((48 + (l >> 3)) * 4, asu(fp)));
      }
      float o[8]; unpack8(av, o);
#pragma unroll
      for (int e = 0; e < 8; ++e) o[e] = l < 48 ? o[e] * fo : 0.f;
      float ov;
      {
        const float t0 = sw32(o[0], o[4]), t1 = sw32(o[1], o[5]), t2 = sw32(o[2], o[6]), t3 = sw32(o[3], o[7]);
        const float c0 = sw16(t0, t2), c1 = sw16(t1, t3);
        const float d0 = c0 + dpp<R8>(c0), d1 = c1 + dpp<R8>(c1);
        ov = rbf((l & 8) ? d1 : d0);
      }
      float y[8] = {0, 0, 0, 0, 0, 0, 0, 0};
#pragma unroll
      for (int i = 0; i < 4; ++i) {
        const int rr = l >> 2;
        const float ow = asf(__builtin_amdgcn_ds_bpermute((32 * (rr & 1) + 16 * (i >> 1) + 8 * (i & 1) + (rr >> 1)) * 4, asu(ov)));
        float f[8]; unpack8(wv[i], f);
#pragma unroll
        for (int e = 0; e < 8; ++e) y[e] = fmaf(ow, f[e], y[e]);
      }
      {
        const float t0 = sw32(y[0], y[4]), t1 = sw32(y[1], y[5]), t2 = sw32(y[2], y[6]), t3 = sw32(y[3], y[7]);
        float c0 = sw16(t0, t2), c1 = sw16(t1, t3);
        c0 += dpp<R4>(c0); c1 += dpp<R4>(c1); c0 += dpp<R8>(c0); c1 += dpp<R8>(c1);
        if ((l & 12) == 0) ((LAS v2f*)(L + L_YP))[kw * 16 + 4 * (l & 3) + (l >> 4)] = v2f{c0, c1};
      }
      if (lds_arrive(F + F_YCNT, l) != 7) RET();
      {
        v4f t = {0.f, 0.f, 0.f, 0.f};
#pragma unroll
        for (int i = 0; i < 8; ++i) t += ((LAS v4f*)(L + L_YP))[i * 8 + (l & 7)];
        __builtin_amdgcn_raw_buffer_store_b64(v2u{nosent(pk(t.x, t.y)), nosent(pk(t.z, t.w))}, rW,
            WS_GR + (l >> 3) * GR_SZ + GR_B + (g * 20 + cb) * 128 + (l & 7) * 8, 0, SC1);
      }
      TS(6);
      if (hl != 0) RET();
      const u32 T0 = F[F_E0], T1 = F[F_E1];
      _Pragma("unroll 1") for (int it = 0; it < SPIN; ++it) {
        FENCE();
        const v4u sg = ld16<SC1>(rW, WS_SIG + min(l, 53) * 128);
        if (__all((sg.x == T0) & (sg.y == T1) & (sg.z == T0) & (sg.w == T1))) break;
        __builtin_amdgcn_s_sleep(4);
      }
      v4u dv[4];
      const int doff = gr + GR_D + s * 0x2000 + (l >> 2) * 128 + (l & 3) * 16;
      _Pragma("unroll 1") for (int it = 0; it < SPIN; ++it) {
        FENCE();
#pragma unroll
        for (int i = 0; i < 4; ++i) dv[i] = ld16<SC1>(rW, (i == 3 && (l >> 2) > 6) ? OOB : doff + i * 2048);
        if (__all(valid4(dv[0]) & valid4(dv[1]) & valid4(dv[2]) & valid4(dv[3]))) break;
      }
      TS(7);
      {
        const bool hrow = (l >> 2) == 6;
        const v4f d3 = __builtin_bit_cast(v4f, dv[3]), z = {0.f, 0.f, 0.f, 0.f};
        const v4f t = __builtin_bit_cast(v4f, dv[0]) + __builtin_bit_cast(v4f, dv[1]) + __builtin_bit_cast(v4f, dv[2]) + (hrow ? z : d3);
        const v4f hh = hrow ? d3 : z;
        float c = sw16(sw32(t.x, t.z), sw32(t.y, t.w)), hc = sw16(sw32(hh.x, hh.z), sw32(hh.y, hh.w));
        c += dpp<R4>(c); hc += dpp<R4>(hc); c += dpp<R8>(c); hc += dpp<R8>(hc);
        const int col = (g * 5 + s) * 16 + 4 * (l & 3) + 2 * (l >> 5) + ((l >> 4) & 1);
        const rsrc_t rOut = mk(a.out, 1280);
        __builtin_amdgcn_raw_buffer_store_b16((unsigned short)pk(hc + rbf(c), 0.f), rOut, (l & 12) == 0 ? col * 2 : OOB, 0, 0);
        if (b == 0) __builtin_amdgcn_raw_buffer_store_b64(v2u{T0, T1}, rW, l == 0 ? WS_E : OOB, 0, SC1);
      }
      TS(8);
    } else {
      kq[4] = ld16<AUX_KV>(r0, off0 + 4 * 8192);
      kq[5] = ld16<AUX_KV>(r0, off0 + 5 * 8192);
      SB();
      const rsrc_t rV = mk(a.V + kvoff, nu * 1024);
      if (kw >= V_EARLY) lds_wait(F + F_VGO, 1);
      v4u vv[6];
#pragma unroll
      for (int i = 0; i < 6; ++i) vv[i] = ld16<AUX_KV>(rV, off0 + i * 8192);
      SB();
      lds_arrive(F + F_VCNT, l);
      TS(1);
      lds_wait(F + F_QRDY, 1);
      const v4u q = ((LAS v4u*)(L + L_Q))[l & 15];
      float sc[6];
#pragma unroll
      for (int i = 0; i < 6; ++i) {
        const float d = dot2(kq[i].w, q.w, dot2(kq[i].z, q.z, dot2(kq[i].y, q.y, dot2(kq[i].x, q.x, 0.f))));
        sc[i] = rbf(rbf(rowsum(d)) * 0.08838834764831845f) * 1.4426950408889634f;
      }
      if (40 + kw >= nu) sc[5] = -INFINITY;
      float m = fmaxf(fmaxf(fmaxf(sc[0], sc[1]), fmaxf(sc[2], sc[3])), fmaxf(sc[4], sc[5]));
      m = mx32(mx16(m));
      if (l == 0) ((LAS float*)(L + L_M))[kw] = m;
      lds_arrive(F + F_MCNT, l);
      lds_wait(F + F_MCNT, 8);
      {
        const v4f m0 = ((LAS v4f*)(L + L_M))[0], m1 = ((LAS v4f*)(L + L_M))[1];
        m = fmaxf(fmaxf(fmaxf(m0.x, m0.y), fmaxf(m0.z, m0.w)), fmaxf(fmaxf(m1.x, m1.y), fmaxf(m1.z, m1.w)));
      }
      float ls = 0.f;
#pragma unroll
      for (int i = 0; i < 6; ++i) { sc[i] = __builtin_amdgcn_exp2f(sc[i] - m); ls += sc[i]; }
      ls = sw16(ls, ls); ls = sw32(ls, ls);
      if (l == 0) ((LAS float*)(L + L_L))[kw] = ls;
      TS(2);
      float acc[8] = {0, 0, 0, 0, 0, 0, 0, 0};
#pragma unroll
      for (int i = 0; i < 6; ++i) {
        float f[8]; unpack8(vv[i], f);
#pragma unroll
        for (int e = 0; e < 8; ++e) acc[e] = fmaf(sc[i], f[e], acc[e]);
        SB();
      }
      TS(3);
      {
        const float t0 = sw32(acc[0], acc[4]), t1 = sw32(acc[1], acc[5]), t2 = sw32(acc[2], acc[6]), t3 = sw32(acc[3], acc[7]);
        ((LAS v2f*)(L + L_OP))[kw * 64 + 4 * (l & 15) + (l >> 4)] = v2f{sw16(t0, t2), sw16(t1, t3)};
      }
      if (lds_arrive(F + F_OCNT, l) != 7) RET();
      v2f o = {0.f, 0.f};
#pragma unroll
      for (int i = 0; i < 8; ++i) o += ((LAS v2f*)(L + L_OP))[i * 64 + l];
      const v4f l0 = ((LAS v4f*)(L + L_L))[0], l1 = ((LAS v4f*)(L + L_L))[1];
      __builtin_amdgcn_raw_buffer_store_b32(nosent(pk(o.x, o.y)), rW, gr + GR_AO + j * 256 + l * 4, 0, SC1);
      const u32 um = nosent(asu(m)), ul = nosent(asu(((l0.x + l0.y) + (l0.z + l0.w)) + ((l1.x + l1.y) + (l1.z + l1.w))));
      __builtin_amdgcn_raw_buffer_store_b128(v4u{um, ul, um, ul}, rW, l == 0 ? gr + GR_AML + j * 128 : OOB, 0, SC1);
      lds_set(F + F_APUB);
      TS(4);
    }
  } else {
    const int jj = j - 24;
    if (jj >= (g < 3 ? 8 : 6) || w > 10) RET();
    const int m = 2 * (g < 3 ? 4 * g : 3 * g + 3) + jj;
    if (w == 10) {
      const v2u e = __builtin_amdgcn_raw_buffer_load_b64(rW, WS_E, 0, SC1);
      const v4u sv = {SENT, SENT, SENT, SENT};
#pragma unroll
      for (int i = 0; i < 5; ++i) {
        const int r = i * 8 + (l >> 3), rg = r / 5;
        __builtin_amdgcn_raw_buffer_store_b128(sv, rW, WS_GR + rg * GR_SZ + GR_D + (r - 5 * rg) * 0x2000 + m * 128 + (l & 7) * 16, 0, SC1);
      }
      __builtin_amdgcn_raw_buffer_store_b128(sv, rW, (m < 40 && l < 8) ? WS_GR + (m / 5) * GR_SZ + GR_D + (m % 5) * 0x2000 + 54 * 128 + l * 16 : OOB, 0, SC1);
      const u64 t = (((u64)e.y << 32) | e.x) + 1;
      const u32 T0 = (u32)t, T1 = (u32)(t >> 32);
      asm volatile("s_waitcnt vmcnt(0)" ::: "memory");
      __builtin_amdgcn_raw_buffer_store_b128(v4u{T0, T1, T0, T1}, rW, l < 8 ? WS_SIG + m * 128 + l * 16 : OOB, 0, SC1);
      nap<DLY_M>();
      _Pragma("unroll 1") for (int it = 0; it < SPIN; ++it) {
        FENCE();
        bool ok = true;
#pragma unroll
        for (int i = 0; i < 3; ++i) {
          const v4u pr = ld16<SC1>(rW, WS_Q + (i * 64 + l) * 1024);
          ok &= (pr.x == T0) & (pr.z == T0) & ((pr.y & 0xffu) == (T1 & 0xffu)) & ((pr.w & 0xffu) == (T1 & 0xffu));
        }
        if (__all(ok)) break;
        __builtin_amdgcn_s_sleep(8);
      }
      FENCE();
      __builtin_amdgcn_s_barrier();
      RET();
    }
    const int col = 64 * w + 8 * (l & 7) + 4 * (l >> 5) + 2 * ((l >> 4) & 1) + ((l >> 3) & 1);
    const float xc = blo(__builtin_amdgcn_raw_buffer_load_b16(mk(a.x, 1280), col * 2, 0, 0));
    nap<DLY_M>();
    const rsrc_t r1 = mk(a.w1 + (size_t)w * 64 * 3456 + m * 64, 63 * 3456 + 64);
    const rsrc_t r2 = mk(a.w2 + (size_t)m * 32 * 1280 + w * 128, 31 * 1280 + 128);
    v4u w1v[4], w2v[4];
#pragma unroll
    for (int i = 0; i < 4; ++i) w1v[i] = ld16<AUX_W1>(r1, (4 * (l >> 2) + i) * 3456 + (l & 3) * 16);
#pragma unroll
    for (int i = 0; i < 4; ++i) w2v[i] = ld16<AUX_W2>(r2, (4 * (l >> 3) + i) * 1280 + (l & 7) * 16);
    SB();
    float f1[4][8], f2[4][8];
#pragma unroll
    for (int i = 0; i < 4; ++i) { unpack8(w1v[i], f1[i]); unpack8(w2v[i], f2[i]); }
#pragma unroll
    for (int i = 0; i < 4; ++i)
#pragma unroll
      for (int e = 0; e < 8; ++e) { asm volatile("" : "+v"(f1[i][e])); asm volatile("" : "+v"(f2[i][e])); }
    TS(1);
    FENCE();
    __builtin_amdgcn_s_barrier();
    FENCE();
    TS(2);
    const int gr = WS_GR + g * GR_SZ;
    const int boff = gr + GR_B + ((l >> 3) * 20 + 2 * w + ((l >> 2) & 1)) * 128 + (l & 3) * 16;
    v4u bv;
    _Pragma("unroll 1") for (int it = 0; it < SPIN; ++it) {
      FENCE();
      bv = ld16<SC1>(rW, boff);
      if (__all(valid4(bv))) break;
    }
    TS(3);
    float hv;
    {
      float y[8]; unpack8(bv, y);
      const float t0 = sw32(y[0], y[4]), t1 = sw32(y[1], y[5]), t2 = sw32(y[2], y[6]), t3 = sw32(y[3], y[7]);
      const float c0 = sw16(t0, t2), c1 = sw16(t1, t3);
      const float d0 = c0 + dpp<R8>(c0), d1 = c1 + dpp<R8>(c1);
      hv = rbf(xc + rbf((l & 8) ? d1 : d0));
    }
    ((LAS float*)(L + L_H))[col] = hv;
    __builtin_amdgcn_raw_buffer_store_b32(nosent(asu(hv)), rW,
        (col >> 4) == m ? WS_GR + (m / 5) * GR_SZ + GR_D + (m % 5) * 0x2000 + 54 * 128 + (col & 15) * 4 : OOB, 0, SC1);
    {
      const float s1 = wavesum(hv), s2 = wavesum(hv * hv);
      if (l == 0) ((LAS v2f*)(L + L_ST))[w] = v2f{s1, s2};
    }
    asm volatile("s_waitcnt lgkmcnt(0)" ::: "memory");
    __builtin_amdgcn_s_barrier();
    FENCE();
    float u[8] = {0, 0, 0, 0, 0, 0, 0, 0};
    {
      const v4f h4 = ((LAS v4f*)(L + L_H))[16 * w + (l >> 2)];
      v4f st = ((LAS v4f*)(L + L_ST))[0];
#pragma unroll
      for (int i = 1; i < 5; ++i) st += ((LAS v4f*)(L + L_ST))[i];
      float mb, rinv; ln_consts(st.x + st.z, st.y + st.w, mb, rinv);
#pragma unroll
      for (int i = 0; i < 4; ++i) {
        const float hn = rbf(rbf(h4[i] - mb) * rinv);
#pragma unroll
        for (int e = 0; e < 8; ++e) u[e] = fmaf(hn, f1[i][e], u[e]);
      }
      const float t0 = sw32(u[0], u[4]), t1 = sw32(u[1], u[5]), t2 = sw32(u[2], u[6]), t3 = sw32(u[3], u[7]);
      float c0 = sw16(t0, t2), c1 = sw16(t1, t3);
      c0 += dpp<R4>(c0); c1 += dpp<R4>(c1); c0 += dpp<R8>(c0); c1 += dpp<R8>(c1);
      if ((l & 12) == 0) ((LAS v2f*)(L + L_P1))[w * 16 + 4 * (l & 3) + (l >> 4)] = v2f{c0, c1};
    }
    asm volatile("s_waitcnt lgkmcnt(0)" ::: "memory");
    __builtin_amdgcn_s_barrier();
    FENCE();
    TS(4);
    {
      v4f t = ((LAS v4f*)(L + L_P1))[l >> 3];
#pragma unroll
      for (int i = 1; i < 10; ++i) t += ((LAS v4f*)(L + L_P1))[i * 8 + (l >> 3)];
      float z[8] = {0, 0, 0, 0, 0, 0, 0, 0};
#pragma unroll
      for (int i = 0; i < 4; ++i) {
        const float r = rbf(t[i]), h1 = r < 0.f ? 0.f : r;
#pragma unroll
        for (int e = 0; e < 8; ++e) z[e] = fmaf(h1, f2[i][e], z[e]);
      }
      const float t0 = sw32(z[0], z[4]), t1 = sw32(z[1], z[5]), t2 = sw32(z[2], z[6]), t3 = sw32(z[3], z[7]);
      const float c0 = sw16(t0, t2), c1 = sw16(t1, t3);
      const float d0 = c0 + dpp<R8>(c0), d1 = c1 + dpp<R8>(c1);
      const int r = col >> 4, rg = r / 5;
      __builtin_amdgcn_raw_buffer_store_b32(nosent(asu((l & 8) ? d1 : d0)), rW,
          WS_GR + rg * GR_SZ + GR_D + (r - 5 * rg) * 0x2000 + m * 128 + (col & 15) * 4, 0, SC1);
    }
    TS(5);
  }
  RET();
}

extern "C" void kernel_launch(void* const* d_in, const int* in_sizes, int n_in, void* d_out, int out_size, void* d_ws, size_t ws_size,
                              hipStream_t stream) {
  Args a;
  a.x = (const char*)d_in[0]; a.K = (const char*)d_in[1]; a.V = (const char*)d_in[2]; a.wq = (const char*)d_in[3];
  a.wo = (const char*)d_in[4]; a.w1 = (const char*)d_in[6]; a.w2 = (const char*)d_in[7];
  a.out = (char*)d_out; a.ws = (char*)d_ws;
  fused_decode_kernel<<<256, 1024, 0, stream>>>(a);
}
```

```cpp
#include <hip/hip_runtime.h>
#include <hip/hip_bf16.h>

typedef unsigned u32;
typedef unsigned long long u64;
typedef u32 v4u __attribute__((ext_vector_type(4)));
typedef u32 v2u __attribute__((ext_vector_type(2)));
typedef float v4f __attribute__((ext_vector_type(4)));
typedef float v2f __attribute__((ext_vector_type(2)));
typedef __bf16 bf16x2 __attribute__((ext_vector_type(2)));
typedef __amdgpu_buffer_rsrc_t rsrc_t;
#define LAS __attribute__((address_space(3)))
#define GAS __attribute__((address_space(1)))

#define AUX_KV 2
#define AUX_WQ 2
#define AUX_WO 2
#define AUX_W1 2
#define AUX_W2 2
#define DLY_Q 12
#define DLY_A 8
#define DLY_M 80
#define V_EARLY 2

#define CODE_BYTES 12288
constexpr int SC1 = 16;
constexpr u32 SENT = 0xFFFFFFFFu;
constexpr int OOB = 0x40000000;
constexpr int SPIN = 1 << 15;
constexpr int WS_E = 0, WS_Q = 0x10000, WS_SIG = 0x40000, WS_GR = 0x80000, GR_SZ = 0x20000;
constexpr int GR_AO = 0, GR_AML = 0x1800, GR_B = 0x4000, GR_D = 0x10000;
constexpr int WS_BYTES = WS_GR + 8 * GR_SZ;
constexpr int WS_TS = 0x800000;
constexpr int L_FLAG = 0;
constexpr int F_XN = 0, F_QCNT = 1, F_VGO = 2, F_QRDY = 3, F_MCNT = 4, F_OCNT = 5, F_YCNT = 6, F_VCNT = 7, F_APUB = 8, F_E0 = 10, F_E1 = 11;
constexpr int L_M = 64;
constexpr int L_L = 96;
constexpr int L_Q = 128;
constexpr int L_XN = 512;
constexpr int L_QP = 512 + 2560;
constexpr int L_OP = L_QP + 4096;
constexpr int L_YP = L_OP + 4096;
constexpr int L_END = L_YP + 1024;
constexpr int L_H = 512;
constexpr int L_ST = 64;
constexpr int L_P1 = L_QP;

#define TS(i) do {} while (0)
#define RET() return

struct Args { const char* x; const char* K; const char* V; const char* wq; const char* wo; const char* w1; const char* w2; char* out; char* ws; };

__device__ __forceinline__ rsrc_t mk(const char* p, int n) { return __builtin_amdgcn_make_buffer_rsrc((void*)p, 0, n, 0x00020000); }
template <int AUX> __device__ __forceinline__ v4u ld16(rsrc_t r, int off) { return __builtin_amdgcn_raw_buffer_load_b128(r, off, 0, AUX); }
__device__ __forceinline__ float asf(u32 x) { return __builtin_bit_cast(float, x); }
__device__ __forceinline__ u32 asu(float x) { return __builtin_bit_cast(u32, x); }
__device__ __forceinline__ float blo(u32 x) { return asf(x << 16); }
__device__ __forceinline__ float bhi(u32 x) { return asf(x & 0xffff0000u); }
__device__ __forceinline__ u32 pk(float lo, float hi) { bf16x2 v = {(__bf16)lo, (__bf16)hi}; return __builtin_bit_cast(u32, v); }
__device__ __forceinline__ float rbf(float x) { return blo(pk(x, 0.f) & 0xffffu); }
__device__ __forceinline__ float dot2(u32 a, u32 b, float c) { return __builtin_amdgcn_fdot2_f32_bf16(__builtin_bit_cast(bf16x2, a), __builtin_bit_cast(bf16x2, b), c, false); }
template <int C> __device__ __forceinline__ float dpp(float x) { return asf(__builtin_amdgcn_mov_dpp(asu(x), C, 0xf, 0xf, true)); }
constexpr int X1 = 0xB1, X2 = 0x4E, HM = 0x141, R4 = 0x124, R8 = 0x128;
__device__ __forceinline__ float sw32(float a, float b) { auto r = __builtin_amdgcn_permlane32_swap(asu(a), asu(b), false, false); return asf(r[0]) + asf(r[1]); }
__device__ __forceinline__ float sw16(float a, float b) { auto r = __builtin_amdgcn_permlane16_swap(asu(a), asu(b), false, false); return asf(r[0]) + asf(r[1]); }
__device__ __forceinline__ float mx32(float a) { auto r = __builtin_amdgcn_permlane32_swap(asu(a), asu(a), false, false); return fmaxf(asf(r[0]), asf(r[1])); }
__device__ __forceinline__ float mx16(float a) { auto r = __builtin_amdgcn_permlane16_swap(asu(a), asu(a), false, false); return fmaxf(asf(r[0]), asf(r[1])); }
__device__ __forceinline__ float rowsum(float x) { x += dpp<X1>(x); x += dpp<X2>(x); x += dpp<HM>(x); x += dpp<R8>(x); return x; }
__device__ __forceinline__ float wavesum(float x) { x = rowsum(x); x = sw16(x, x); return sw32(x, x); }
__device__ __forceinline__ void unpack8(v4u v, float (&f)[8]) {
  f[0] = blo(v.x); f[1] = bhi(v.x); f[2] = blo(v.y); f[3] = bhi(v.y); f[4] = blo(v.z); f[5] = bhi(v.z); f[6] = blo(v.w); f[7] = bhi(v.w);
}
__device__ __forceinline__ bool valid4(v4u v) { return (v.x != SENT) & (v.y != SENT) & (v.z != SENT) & (v.w != SENT); }
__device__ __forceinline__ u32 nosent(u32 x) { return x < 0xFFFFFFFEu ? x : 0xFFFFFFFEu; }
#define FENCE() asm volatile("" ::: "memory")
#define SB() __builtin_amdgcn_sched_barrier(0)
template <int N> __device__ __forceinline__ void nap() {
#pragma unroll
  for (int i = 0; i < N / 100; ++i) __builtin_amdgcn_s_sleep(100);
  if (N % 100) __builtin_amdgcn_s_sleep(N % 100);
}
__device__ __forceinline__ void lds_wait(LAS u32* f, u32 want) {
  _Pragma("unroll 1") for (int i = 0; i < (SPIN << 3); ++i) {
    if (__builtin_amdgcn_readfirstlane(__hip_atomic_load(f, __ATOMIC_RELAXED, __HIP_MEMORY_SCOPE_WORKGROUP)) >= want) break;
    __builtin_amdgcn_s_sleep(1);
  }
  FENCE();
}
__device__ __forceinline__ void lds_set(LAS u32* f) { FENCE(); __hip_atomic_store(f, 1u, __ATOMIC_RELAXED, __HIP_MEMORY_SCOPE_WORKGROUP); FENCE(); }
__device__ __forceinline__ u32 lds_arrive(LAS u32* f, int l) {
  FENCE();
  u32 old = 0;
  if (l == 0) old = __hip_atomic_fetch_add(f, 1u, __ATOMIC_RELAXED, __HIP_MEMORY_SCOPE_WORKGROUP);
  old = __builtin_amdgcn_readfirstlane(old);
  FENCE();
  return old;
}
__device__ __forceinline__ void ln_consts(float s1, float s2, float& mb, float& rinv) {
  const float mean = s1 * (1.f / 640.f);
  const float var = fmaxf(s2 * (1.f / 640.f) - mean * mean, 0.f);
  mb = rbf(mean);
  rinv = 1.f / rbf(__builtin_amdgcn_sqrtf(rbf(rbf(var) + 1e-5f)));
}

__global__ void __launch_bounds__(1024) fused_decode_kernel(Args a) {
  __shared__ __attribute__((aligned(16))) char smem[L_END];
  LAS char* L = (LAS char*)smem;
  LAS u32* F = (LAS u32*)smem;
  const int b = blockIdx.x, l = threadIdx.x & 63, w = __builtin_amdgcn_readfirstlane(threadIdx.x >> 6);
  const int g = b & 7, j = b >> 3;
  u32 tsv = 0, pt0 = 0, pt1 = 0; u64 pc0 = 0; (void)tsv; (void)pt0; (void)pt1; (void)pc0;
  u64 pc; asm volatile("s_getpc_b64 %0" : "=s"(pc));
  TS(0);
  const rsrc_t rW = mk(a.ws, WS_BYTES);
  if (j >= 24 && w >= 14) {
    const rsrc_t rC = mk((const char*)(pc & ~255ull), CODE_BYTES);
    u32 v = __builtin_amdgcn_raw_buffer_load_b32(rC, ((w - 14) * 64 + l) * 128, 0, 0);
    asm volatile("s_waitcnt vmcnt(0)" :: "v"(v) : "memory");
    RET();
  }
  if (__builtin_expect(j < 24, 1)) {
    const int hl = j / 6, s = j - 6 * hl, h = 4 * g + hl;
    const bool isQ = w < 8;
    const int kw = w & 7;
    const int u0 = 43 * s - (s > 4 ? 1 : 0), nu = s < 4 ? 43 : 42;
    const int uq0 = 27 * s - (s > 4 ? 1 : 0), nuq = s < 4 ? 27 : 26;
    const size_t kvoff = ((size_t)h * 4096 + 3072 + 4 * u0) * 256;
    const rsrc_t r0 = mk(isQ ? a.wq + h * 256 + (size_t)uq0 * 32768 : a.K + kvoff, isQ ? (4 * nuq - 1) * 8192 + 256 : nu * 1024);
    const int step = isQ ? 262144 : 8192;
    const int off0 = isQ ? (kw * 4 + (l >> 4)) * 8192 + (l & 15) * 16 : kw * 1024 + l * 16;
    v4u xa = {0, 0, 0, 0}, xb = {0, 0, 0, 0};
    if (w == 7) { const rsrc_t rx = mk(a.x, 1280); xa = ld16<0>(rx, l * 16); xb = ld16<0>(rx, 1024 + l * 16); }
    v4u kq[6];
    static_assert(AUX_WQ == AUX_KV, "shared first loads");
    kq[0] = ld16<AUX_KV>(r0, off0);
    kq[1] = ld16<AUX_KV>(r0, off0 + step);
    SB();
    if (w == 15 && l < 16) F[l] = 0;
    asm volatile("s_waitcnt lgkmcnt(0)" ::: "memory");
    __builtin_amdgcn_s_barrier();
    SB();
    kq[2] = ld16<AUX_KV>(r0, off0 + 2 * step);
    kq[3] = ld16<AUX_KV>(r0, off0 + 3 * step);
    SB();
    const int gr = WS_GR + g * GR_SZ;
    if (isQ) {
      if (w == 6) {
        const v2u e = __builtin_amdgcn_raw_buffer_load_b64(rW, WS_E, 0, SC1);
        const v4u sv = {SENT, SENT, SENT, SENT};
        __builtin_amdgcn_raw_buffer_store_b128(sv, rW, l < 16 ? gr + GR_AO + j * 256 + l * 16 : l < 24 ? gr + GR_AML + j * 128 + (l - 16) * 16 : OOB, 0, SC1);
        __builtin_amdgcn_raw_buffer_store_b128(sv, rW, s < 5 ? WS_GR + (l >> 3) * GR_SZ + GR_B + (g * 20 + hl * 5 + s) * 128 + (l & 7) * 16 : OOB, 0, SC1);
        const u64 t = (((u64)e.y << 32) | e.x) + 1;
        asm volatile("s_waitcnt vmcnt(0)" ::: "memory");
        if (l == 0) { F[F_E0] = (u32)t; F[F_E1] = (u32)(t >> 32); }
      }
      if (w == 7) {
        float f[8], e[8];
        unpack8(xa, f); unpack8(xb, e);
        float s1 = 0.f, s2 = 0.f;
#pragma unroll
        for (int i = 0; i < 8; ++i) { s1 += f[i] + e[i]; s2 = fmaf(f[i], f[i], fmaf(e[i], e[i], s2)); }
        s1 = wavesum(s1); s2 = wavesum(s2);
        float mb, rinv; ln_consts(s1, s2, mb, rinv);
#pragma unroll
        for (int i = 0; i < 8; ++i) { f[i] = rbf(rbf(f[i] - mb) * rinv); e[i] = rbf(rbf(e[i] - mb) * rinv); }
        LAS v4f* xn = (LAS v4f*)(L + L_XN);
        xn[2 * l] = v4f{f[0], f[1], f[2], f[3]}; xn[2 * l + 1] = v4f{f[4], f[5], f[6], f[7]};
        if (l < 16) { xn[128 + 2 * l] = v4f{e[0], e[1], e[2], e[3]}; xn[129 + 2 * l] = v4f{e[4], e[5], e[6], e[7]}; }
        lds_set(F + F_XN);
      }
      lds_wait(F + F_XN, 1);
      float acc[8] = {0, 0, 0, 0, 0, 0, 0, 0};
#pragma unroll
      for (int i = 0; i < 4; ++i) {
        const int row = min(4 * (uq0 + i * 8 + kw) + (l >> 4), 639);
        const float xv = ((LAS float*)(L + L_XN))[row];
        float f[8]; unpack8(kq[i], f);
#pragma unroll
        for (int e = 0; e < 8; ++e) acc[e] = fmaf(xv, f[e], acc[e]);
      }
      {
        const float t0 = sw32(acc[0], acc[4]), t1 = sw32(acc[1], acc[5]), t2 = sw32(acc[2], acc[6]), t3 = sw32(acc[3], acc[7]);
        ((LAS v2f*)(L + L_QP))[kw * 64 + 4 * (l & 15) + (l >> 4)] = v2f{sw16(t0, t2), sw16(t1, t3)};
      }
      TS(1);
      if (lds_arrive(F + F_QCNT, l) == 7) {
        v2f q = {0.f, 0.f};
#pragma unroll
        for (int i = 0; i < 8; ++i) q += ((LAS v2f*)(L + L_QP))[i * 64 + l];
        const u32 T0 = F[F_E0], T1 = F[F_E1] & 0xffu;
        __builtin_amdgcn_raw_buffer_store_b128(v4u{T0, (asu(q.x) & ~0xffu) | T1, T0, (asu(q.y) & ~0xffu) | T1}, rW, WS_Q + (h * 6 + s) * 1024 + l * 16, 0, SC1);
        TS(2);
        nap<DLY_Q>();
        float q0, q1;
        _Pragma("unroll 1") for (int it = 0; it < SPIN; ++it) {
          FENCE();
          v4u v[6];
#pragma unroll
          for (int p = 0; p < 6; ++p) v[p] = ld16<SC1>(rW, WS_Q + (h * 6 + p) * 1024 + l * 16);
          const v4u pr = ld16<SC1>(rW, WS_Q + (g * 24 + min(l, 23)) * 1024);
          SB();
          if (it == 0) lds_set(F + F_VGO);
          SB();
          bool ok = (pr.x == T0) & (pr.z == T0) & ((pr.y & 0xffu) == T1) & ((pr.w & 0xffu) == T1);
          q0 = 0.f; q1 = 0.f;
#pragma unroll
          for (int p = 0; p < 6; ++p) {
            ok &= (v[p].x == T0) & (v[p].z == T0) & ((v[p].y & 0xffu) == T1) & ((v[p].w & 0xffu) == T1);
            q0 += asf(v[p].y & ~0xffu); q1 += asf(v[p].w & ~0xffu);
          }
          if (__all(ok)) break;
        }
        ((LAS u32*)(L + L_Q))[l] = pk(q0, q1);
        lds_set(F + F_QRDY);
        TS(3);
      }
      if (s == 5) RET();
      const int cb = hl * 5 + s;
      const rsrc_t rO = mk(a.wo + (size_t)(g * 512 + kw * 64) * 1280 + cb * 64, 63 * 1280 + 64);
      lds_wait(F + F_VCNT, 8);
      v4u wv[4];
#pragma unroll
      for (int i = 0; i < 4; ++i) wv[i] = ld16<AUX_WO>(rO, (4 * (l >> 2) + i) * 1280 + (l & 3) * 16);
      SB();
      lds_wait(F + F_QRDY, 1);
      lds_wait(F + F_APUB, 1);
      nap<DLY_A>();
      TS(4);
      const int aoff = l < 48 ? gr + GR_AO + ((kw >> 1) * 6 + (l >> 3)) * 256 + (kw & 1) * 128 + (l & 7) * 16
                              : l < 54 ? gr + GR_AML + ((kw >> 1) * 6 + l - 48) * 128 : OOB;
      v4u av;
      _Pragma("unroll 1") for (int it = 0; it < SPIN; ++it) {
        FENCE();
        av = ld16<SC1>(rW, aoff);
        if (__all(valid4(av))) break;
      }
      TS(5);
      float fo;
      {
        const bool ml = l >= 48 && l < 54;
        const float m = ml ? asf(av.x) : -INFINITY, ls = ml ? asf(av.y) : 0.f;
        float M = fmaxf(m, dpp<X1>(m)); M = fmaxf(M, dpp<X2>(M)); M = fmaxf(M, dpp<HM>(M));
        const float al = __builtin_amdgcn_exp2f(m - M);
        float Ls = al * ls; Ls += dpp<X1>(Ls); Ls += dpp<X2>(Ls); Ls += dpp<HM>(Ls);
        const float fp = ml ? al * __builtin_amdgcn_rcpf(Ls) : 0.f;
        fo = asf(__builtin_amdgcn_ds_bpermute((48 + (l >> 3)) * 4, asu(fp)));
      }
      float o[8]; unpack8(av, o);
#pragma unroll
      for (int e = 0; e < 8; ++e) o[e] = l < 48 ? o[e] * fo : 0.f;
      float ov;
      {
        const float t0 = sw32(o[0], o[4]), t1 = sw32(o[1], o[5]), t2 = sw32(o[2], o[6]), t3 = sw32(o[3], o[7]);
        const float c0 = sw16(t0, t2), c1 = sw16(t1, t3);
        const float d0 = c0 + dpp<R8>(c0), d1 = c1 + dpp<R8>(c1);
        ov = rbf((l & 8) ? d1 : d0);
      }
      float y[8] = {0, 0, 0, 0, 0, 0, 0, 0};
#pragma unroll
      for (int i = 0; i < 4; ++i) {
        const int rr = l >> 2;
        const float ow = asf(__builtin_amdgcn_ds_bpermute((32 * (rr & 1) + 16 * (i >> 1) + 8 * (i & 1) + (rr >> 1)) * 4, asu(ov)));
        float f[8]; unpack8(wv[i], f);
#pragma unroll
        for (int e = 0; e < 8; ++e) y[e] = fmaf(ow, f[e], y[e]);
      }
      {
        const float t0 = sw32(y[0], y[4]), t1 = sw32(y[1], y[5]), t2 = sw32(y[2], y[6]), t3 = sw32(y[3], y[7]);
        float c0 = sw16(t0, t2), c1 = sw16(t1, t3);
        c0 += dpp<R4>(c0); c1 += dpp<R4>(c1); c0 += dpp<R8>(c0); c1 += dpp<R8>(c1);
        if ((l & 12) == 0) ((LAS v2f*)(L + L_YP))[kw * 16 + 4 * (l & 3) + (l >> 4)] = v2f{c0, c1};
      }
      if (lds_arrive(F + F_YCNT, l) != 7) RET();
      {
        v4f t = {0.f, 0.f, 0.f, 0.f};
#pragma unroll
        for (int i = 0; i < 8; ++i) t += ((LAS v4f*)(L + L_YP))[i * 8 + (l & 7)];
        __builtin_amdgcn_raw_buffer_store_b64(v2u{nosent(pk(t.x, t.y)), nosent(pk(t.z, t.w))}, rW,
            WS_GR + (l >> 3) * GR_SZ + GR_B + (g * 20 + cb) * 128 + (l & 7) * 8, 0, SC1);
      }
      TS(6);
      if (hl != 0) RET();
      const u32 T0 = F[F_E0], T1 = F[F_E1];
      _Pragma("unroll 1") for (int it = 0; it < SPIN; ++it) {
        FENCE();
        const v4u sg = ld16<SC1>(rW, WS_SIG + min(l, 53) * 128);
        if (__all((sg.x == T0) & (sg.y == T1) & (sg.z == T0) & (sg.w == T1))) break;
        __builtin_amdgcn_s_sleep(4);
      }
      v4u dv[4];
      const int doff = gr + GR_D + s * 0x2000 + (l >> 2) * 128 + (l & 3) * 16;
      _Pragma("unroll 1") for (int it = 0; it < SPIN; ++it) {
        FENCE();
#pragma unroll
        for (int i = 0; i < 4; ++i) dv[i] = ld16<SC1>(rW, (i == 3 && (l >> 2) > 6) ? OOB : doff + i * 2048);
        if (__all(valid4(dv[0]) & valid4(dv[1]) & valid4(dv[2]) & valid4(dv[3]))) break;
      }
      TS(7);
      {
        const bool hrow = (l >> 2) == 6;
        const v4f d3 = __builtin_bit_cast(v4f, dv[3]), z = {0.f, 0.f, 0.f, 0.f};
        const v4f t = __builtin_bit_cast(v4f, dv[0]) + __builtin_bit_cast(v4f, dv[1]) + __builtin_bit_cast(v4f, dv[2]) + (hrow ? z : d3);
        const v4f hh = hrow ? d3 : z;
        float c = sw16(sw32(t.x, t.z), sw32(t.y, t.w)), hc = sw16(sw32(hh.x, hh.z), sw32(hh.y, hh.w));
        c += dpp<R4>(c); hc += dpp<R4>(hc); c += dpp<R8>(c); hc += dpp<R8>(hc);
        const int col = (g * 5 + s) * 16 + 4 * (l & 3) + 2 * (l >> 5) + ((l >> 4) & 1);
        const rsrc_t rOut = mk(a.out, 1280);
        __builtin_amdgcn_raw_buffer_store_b16((unsigned short)pk(hc + rbf(c), 0.f), rOut, (l & 12) == 0 ? col * 2 : OOB, 0, 0);
        if (b == 0) __builtin_amdgcn_raw_buffer_store_b64(v2u{T0, T1}, rW, l == 0 ? WS_E : OOB, 0, SC1);
      }
      TS(8);
    } else {
      kq[4] = ld16<AUX_KV>(r0, off0 + 4 * 8192);
      kq[5] = ld16<AUX_KV>(r0, off0 + 5 * 8192);
      SB();
      const rsrc_t rV = mk(a.V + kvoff, nu * 1024);
      if (kw >= V_EARLY) lds_wait(F + F_VGO, 1);
      v4u vv[6];
#pragma unroll
      for (int i = 0; i < 6; ++i) vv[i] = ld16<AUX_KV>(rV, off0 + i * 8192);
      SB();
      lds_arrive(F + F_VCNT, l);
      TS(1);
      lds_wait(F + F_QRDY, 1);
      const v4u q = ((LAS v4u*)(L + L_Q))[l & 15];
      float sc[6];
#pragma unroll
      for (int i = 0; i < 6; ++i) {
        const float d = dot2(kq[i].w, q.w, dot2(kq[i].z, q.z, dot2(kq[i].y, q.y, dot2(kq[i].x, q.x, 0.f))));
        sc[i] = rbf(rbf(rowsum(d)) * 0.08838834764831845f) * 1.4426950408889634f;
      }
      if (40 + kw >= nu) sc[5] = -INFINITY;
      float m = fmaxf(fmaxf(fmaxf(sc[0], sc[1]), fmaxf(sc[2], sc[3])), fmaxf(sc[4], sc[5]));
      m = mx32(mx16(m));
      if (l == 0) ((LAS float*)(L + L_M))[kw] = m;
      lds_arrive(F + F_MCNT, l);
      lds_wait(F + F_MCNT, 8);
      {
        const v4f m0 = ((LAS v4f*)(L + L_M))[0], m1 = ((LAS v4f*)(L + L_M))[1];
        m = fmaxf(fmaxf(fmaxf(m0.x, m0.y), fmaxf(m0.z, m0.w)), fmaxf(fmaxf(m1.x, m1.y), fmaxf(m1.z, m1.w)));
      }
      float ls = 0.f;
#pragma unroll
      for (int i = 0; i < 6; ++i) { sc[i] = __builtin_amdgcn_exp2f(sc[i] - m); ls += sc[i]; }
      ls = sw16(ls, ls); ls = sw32(ls, ls);
      if (l == 0) ((LAS float*)(L + L_L))[kw] = ls;
      TS(2);
      float acc[8] = {0, 0, 0, 0, 0, 0, 0, 0};
#pragma unroll
      for (int i = 0; i < 6; ++i) {
        float f[8]; unpack8(vv[i], f);
#pragma unroll
        for (int e = 0; e < 8; ++e) acc[e] = fmaf(sc[i], f[e], acc[e]);
        SB();
      }
      TS(3);
      {
        const float t0 = sw32(acc[0], acc[4]), t1 = sw32(acc[1], acc[5]), t2 = sw32(acc[2], acc[6]), t3 = sw32(acc[3], acc[7]);
        ((LAS v2f*)(L + L_OP))[kw * 64 + 4 * (l & 15) + (l >> 4)] = v2f{sw16(t0, t2), sw16(t1, t3)};
      }
      if (lds_arrive(F + F_OCNT, l) != 7) RET();
      v2f o = {0.f, 0.f};
#pragma unroll
      for (int i = 0; i < 8; ++i) o += ((LAS v2f*)(L + L_OP))[i * 64 + l];
      const v4f l0 = ((LAS v4f*)(L + L_L))[0], l1 = ((LAS v4f*)(L + L_L))[1];
      __builtin_amdgcn_raw_buffer_store_b32(nosent(pk(o.x, o.y)), rW, gr + GR_AO + j * 256 + l * 4, 0, SC1);
      const u32 um = nosent(asu(m)), ul = nosent(asu(((l0.x + l0.y) + (l0.z + l0.w)) + ((l1.x + l1.y) + (l1.z + l1.w))));
      __builtin_amdgcn_raw_buffer_store_b128(v4u{um, ul, um, ul}, rW, l == 0 ? gr + GR_AML + j * 128 : OOB, 0, SC1);
      lds_set(F + F_APUB);
      TS(4);
    }
  } else {
    const int jj = j - 24;
    if (jj >= (g < 3 ? 8 : 6) || w > 10) RET();
    const int m = 2 * (g < 3 ? 4 * g : 3 * g + 3) + jj;
    if (w == 10) {
      const v2u e = __builtin_amdgcn_raw_buffer_load_b64(rW, WS_E, 0, SC1);
      const v4u sv = {SENT, SENT, SENT, SENT};
#pragma unroll
      for (int i = 0; i < 5; ++i) {
        const int r = i * 8 + (l >> 3), rg = r / 5;
        __builtin_amdgcn_raw_buffer_store_b128(sv, rW, WS_GR + rg * GR_SZ + GR_D + (r - 5 * rg) * 0x2000 + m * 128 + (l & 7) * 16, 0, SC1);
      }
      __builtin_amdgcn_raw_buffer_store_b128(sv, rW, (m < 40 && l < 8) ? WS_GR + (m / 5) * GR_SZ + GR_D + (m % 5) * 0x2000 + 54 * 128 + l * 16 : OOB, 0, SC1);
      const u64 t = (((u64)e.y << 32) | e.x) + 1;
      const u32 T0 = (u32)t, T1 = (u32)(t >> 32);
      asm volatile("s_waitcnt vmcnt(0)" ::: "memory");
      __builtin_amdgcn_raw_buffer_store_b128(v4u{T0, T1, T0, T1}, rW, l < 8 ? WS_SIG + m * 128 + l * 16 : OOB, 0, SC1);
      nap<DLY_M>();
      _Pragma("unroll 1") for (int it = 0; it < SPIN; ++it) {
        FENCE();
        bool ok = true;
#pragma unroll
        for (int i = 0; i < 3; ++i) {
          const v4u pr = ld16<SC1>(rW, WS_Q + (i * 64 + l) * 1024);
          ok &= (pr.x == T0) & (pr.z == T0) & ((pr.y & 0xffu) == (T1 & 0xffu)) & ((pr.w & 0xffu) == (T1 & 0xffu));
        }
        if (__all(ok)) break;
        __builtin_amdgcn_s_sleep(8);
      }
      FENCE();
      __builtin_amdgcn_s_barrier();
      RET();
    }
    const int col = 64 * w + 8 * (l & 7) + 4 * (l >> 5) + 2 * ((l >> 4) & 1) + ((l >> 3) & 1);
    const float xc = blo(__builtin_amdgcn_raw_buffer_load_b16(mk(a.x, 1280), col * 2, 0, 0));
    nap<DLY_M>();
    const rsrc_t r1 = mk(a.w1 + (size_t)w * 64 * 3456 + m * 64, 63 * 3456 + 64);
    const rsrc_t r2 = mk(a.w2 + (size_t)m * 32 * 1280 + w * 128, 31 * 1280 + 128);
    v4u w1v[4], w2v[4];
#pragma unroll
    for (int i = 0; i < 4; ++i) w1v[i] = ld16<AUX_W1>(r1, (4 * (l >> 2) + i) * 3456 + (l & 3) * 16);
#pragma unroll
    for (int i = 0; i < 4; ++i) w2v[i] = ld16<AUX_W2>(r2, (4 * (l >> 3) + i) * 1280 + (l & 7) * 16);
    SB();
    float f1[4][8], f2[4][8];
#pragma unroll
    for (int i = 0; i < 4; ++i) { unpack8(w1v[i], f1[i]); unpack8(w2v[i], f2[i]); }
#pragma unroll
    for (int i = 0; i < 4; ++i)
#pragma unroll
      for (int e = 0; e < 8; ++e) { asm volatile("" : "+v"(f1[i][e])); asm volatile("" : "+v"(f2[i][e])); }
    TS(1);
    FENCE();
    __builtin_amdgcn_s_barrier();
    FENCE();
    TS(2);
    const int gr = WS_GR + g * GR_SZ;
    const int boff = gr + GR_B + ((l >> 3) * 20 + 2 * w + ((l >> 2) & 1)) * 128 + (l & 3) * 16;
    v4u bv;
    _Pragma("unroll 1") for (int it = 0; it < SPIN; ++it) {
      FENCE();
      bv = ld16<SC1>(rW, boff);
      if (__all(valid4(bv))) break;
    }
    TS(3);
    float hv;
    {
      float y[8]; unpack8(bv, y);
      const float t0 = sw32(y[0], y[4]), t1 = sw32(y[1], y[5]), t2 = sw32(y[2], y[6]), t3 = sw32(y[3], y[7]);
      const float c0 = sw16(t0, t2), c1 = sw16(t1, t3);
      const float d0 = c0 + dpp<R8>(c0), d1 = c1 + dpp<R8>(c1);
      hv = rbf(xc + rbf((l & 8) ? d1 : d0));
    }
    ((LAS float*)(L + L_H))[col] = hv;
    __builtin_amdgcn_raw_buffer_store_b32(nosent(asu(hv)), rW,
        (col >> 4) == m ? WS_GR + (m / 5) * GR_SZ + GR_D + (m % 5) * 0x2000 + 54 * 128 + (col & 15) * 4 : OOB, 0, SC1);
    {
      const float s1 = wavesum(hv), s2 = wavesum(hv * hv);
      if (l == 0) ((LAS v2f*)(L + L_ST))[w] = v2f{s1, s2};
    }
    asm volatile("s_waitcnt lgkmcnt(0)" ::: "memory");
    __builtin_amdgcn_s_barrier();
    FENCE();
    float u[8] = {0, 0, 0, 0, 0, 0, 0, 0};
    {
      const v4f h4 = ((LAS v4f*)(L + L_H))[16 * w + (l >> 2)];
      v4f st = ((LAS v4f*)(L + L_ST))[0];
#pragma unroll
      for (int i = 1; i < 5; ++i) st += ((LAS v4f*)(L + L_ST))[i];
      float mb, rinv; ln_consts(st.x + st.z, st.y + st.w, mb, rinv);
#pragma unroll
      for (int i = 0; i < 4; ++i) {
        const float hn = rbf(rbf(h4[i] - mb) * rinv);
#pragma unroll
        for (int e = 0; e < 8; ++e) u[e] = fmaf(hn, f1[i][e], u[e]);
      }
      const float t0 = sw32(u[0], u[4]), t1 = sw32(u[1], u[5]), t2 = sw32(u[2], u[6]), t3 = sw32(u[3], u[7]);
      float c0 = sw16(t0, t2), c1 = sw16(t1, t3);
      c0 += dpp<R4>(c0); c1 += dpp<R4>(c1); c0 += dpp<R8>(c0); c1 += dpp<R8>(c1);
      if ((l & 12) == 0) ((LAS v2f*)(L + L_P1))[w * 16 + 4 * (l & 3) + (l >> 4)] = v2f{c0, c1};
    }
    asm volatile("s_waitcnt lgkmcnt(0)" ::: "memory");
    __builtin_amdgcn_s_barrier();
    FENCE();
    TS(4);
    {
      v4f t = ((LAS v4f*)(L + L_P1))[l >> 3];
#pragma unroll
      for (int i = 1; i < 10; ++i) t += ((LAS v4f*)(L + L_P1))[i * 8 + (l >> 3)];
      float z[8] = {0, 0, 0, 0, 0, 0, 0, 0};
#pragma unroll
      for (int i = 0; i < 4; ++i) {
        const float r = rbf(t[i]), h1 = r < 0.f ? 0.f : r;
#pragma unroll
        for (int e = 0; e < 8; ++e) z[e] = fmaf(h1, f2[i][e], z[e]);
      }
      const float t0 = sw32(z[0], z[4]), t1 = sw32(z[1], z[5]), t2 = sw32(z[2], z[6]), t3 = sw32(z[3], z[7]);
      const float c0 = sw16(t0, t2), c1 = sw16(t1, t3);
      const float d0 = c0 + dpp<R8>(c0), d1 = c1 + dpp<R8>(c1);
      const int r = col >> 4, rg = r / 5;
      __builtin_amdgcn_raw_buffer_store_b32(nosent(asu((l & 8) ? d1 : d0)), rW,
          WS_GR + rg * GR_SZ + GR_D + (r - 5 * rg) * 0x2000 + m * 128 + (col & 15) * 4, 0, SC1);
    }
    TS(5);
  }
  RET();
}

__global__ void k_stamp(char* ws){ if(blockIdx.x==0 && threadIdx.x==0){ unsigned long long t; asm volatile("s_memrealtime %0\n\ts_waitcnt lgkmcnt(0)" : "=s"(t) :: "memory"); __hip_atomic_store((unsigned long long*)(ws+0x700000),t,__ATOMIC_RELAXED,__HIP_MEMORY_SCOPE_AGENT);} }
__global__ void k_spin(char* ws){ unsigned long long t0=__hip_atomic_load((unsigned long long*)(ws+0x700000),__ATOMIC_RELAXED,__HIP_MEMORY_SCOPE_AGENT); for(int i=0;i<1000000;i++){ unsigned long long t; asm volatile("s_memrealtime %0\n\ts_waitcnt lgkmcnt(0)" : "=s"(t) :: "memory"); if(t-t0>=6000) break; } }
extern "C" void kernel_launch(void* const* d_in, const int* in_sizes, int n_in, void* d_out, int out_size, void* d_ws, size_t ws_size,
                              hipStream_t stream) {
  Args a;
  a.x = (const char*)d_in[0]; a.K = (const char*)d_in[1]; a.V = (const char*)d_in[2]; a.wq = (const char*)d_in[3];
  a.wo = (const char*)d_in[4]; a.w1 = (const char*)d_in[6]; a.w2 = (const char*)d_in[7];
  a.out = (char*)d_out; a.ws = (char*)d_ws;
  k_stamp<<<256,64,0,stream>>>((char*)d_ws);
  fused_decode_kernel<<<256, 1024, 0, stream>>>(a);
  k_spin<<<1,64,0,stream>>>((char*)d_ws);
}
```

```cpp
#define DLY_K 0
#define POL_W ""
#include <hip/hip_runtime.h>
#include <hip/hip_bf16.h>

typedef unsigned u32;
typedef unsigned long long u64;
typedef u32 v4u __attribute__((ext_vector_type(4)));
typedef u32 v2u __attribute__((ext_vector_type(2)));
typedef float v4f __attribute__((ext_vector_type(4)));
typedef float v2f __attribute__((ext_vector_type(2)));
typedef short v4s __attribute__((ext_vector_type(4)));
typedef __bf16 bf16x2 __attribute__((ext_vector_type(2)));
typedef __bf16 bf16x8 __attribute__((ext_vector_type(8)));
typedef __amdgpu_buffer_rsrc_t rsrc_t;
#define LAS __attribute__((address_space(3)))
#define GAS __attribute__((address_space(1)))
#define CAS __attribute__((address_space(4)))

#define AUX_S 2
#define DLY_Q 45
#define DLY_A 6
#define DLY_M 115
#define V_EARLY 1
#define PRIO_Q 0
#define BELL_Q 0
#define WO_AFTER_Q 1
#define K_EARLY 5
#define V_TAIL 0
#define V_TAIL_UNUSED 0
#define WO_EARLY 4
#define CODE_BYTES 10240

constexpr int SC1 = 16;
constexpr u32 SENT = 0xFFFFFFFFu;
constexpr int OOB = 0x40000000;
#define SPINLOG 15
constexpr int SPIN = 1 << SPINLOG;
constexpr int WS_E = 0, WS_Q = 0x10000, WS_SA = 0x40000, WS_SM = 0x48000, WS_GR = 0x80000, GR_SZ = 0x20000;
constexpr int GR_A = 0, GR_B = 0x4000, GR_D = 0x10000;
constexpr int WS_BYTES = WS_GR + 8 * GR_SZ;
constexpr int WS_TS = 0x800000;
constexpr int F_VGO = 0, F_QRDY = 1, F_OCNT = 3, F_YCNT = 4, F_VCNT = 5, F_APUB = 6, F_PRDY = 7, F_QCNT = 2, F_TAG = 8;
constexpr int L_E = 64;
constexpr int L_M = 80, L_L = 96;
constexpr int L_Q = 128;
constexpr int L_QP = 512;
constexpr int L_OP = L_QP + 2048;
constexpr int L_YP = L_OP + 2048;
constexpr int L_IMG = 5120;
constexpr int L_END = L_IMG + 81920;
constexpr int L_ST = 0;
constexpr int L_SX = 32;
constexpr int L_P1 = 128;
constexpr int L_XW = 640;
constexpr int L_CS = 1152;

#define TS(i) do {} while (0)
#define RET() return

struct Srd { const char* p; u32 n; u32 f; };
struct Args { Srd t[9]; };

template <int AUX> __device__ __forceinline__ v4u ld16(rsrc_t r, int off, int soff = 0) { return __builtin_amdgcn_raw_buffer_load_b128(r, off, soff, AUX); }
__device__ __forceinline__ float asf(u32 x) { return __builtin_bit_cast(float, x); }
__device__ __forceinline__ u32 asu(float x) { return __builtin_bit_cast(u32, x); }
__device__ __forceinline__ float blo(u32 x) { return asf(x << 16); }
__device__ __forceinline__ float bhi(u32 x) { return asf(x & 0xffff0000u); }
__device__ __forceinline__ u32 pk(float lo, float hi) { bf16x2 v = {(__bf16)lo, (__bf16)hi}; return __builtin_bit_cast(u32, v); }
__device__ __forceinline__ float rbf(float x) { return blo(pk(x, 0.f)); }
__device__ __forceinline__ float dot2(u32 a, u32 b, float c) { return __builtin_amdgcn_fdot2_f32_bf16(__builtin_bit_cast(bf16x2, a), __builtin_bit_cast(bf16x2, b), c, false); }
__device__ __forceinline__ v4f mfma(v4u a, v4u b, v4f c) { return __builtin_amdgcn_mfma_f32_16x16x32_bf16(__builtin_bit_cast(bf16x8, a), __builtin_bit_cast(bf16x8, b), c, 0, 0, 0); }
template <int C> __device__ __forceinline__ u32 dppu(u32 x) { return __builtin_amdgcn_mov_dpp(x, C, 0xf, 0xf, true); }
template <int C> __device__ __forceinline__ float dpp(float x) { return asf(dppu<C>(asu(x))); }
constexpr int X1 = 0xB1, X2 = 0x4E, HM = 0x141, R4 = 0x124, R8 = 0x128, BC = 0x150;
__device__ __forceinline__ float sw32(float a, float b) { auto r = __builtin_amdgcn_permlane32_swap(asu(a), asu(b), false, false); return asf(r[0]) + asf(r[1]); }
__device__ __forceinline__ float sw16(float a, float b) { auto r = __builtin_amdgcn_permlane16_swap(asu(a), asu(b), false, false); return asf(r[0]) + asf(r[1]); }
__device__ __forceinline__ float mx32(float a) { auto r = __builtin_amdgcn_permlane32_swap(asu(a), asu(a), false, false); return fmaxf(asf(r[0]), asf(r[1])); }
__device__ __forceinline__ float mx16(float a) { auto r = __builtin_amdgcn_permlane16_swap(asu(a), asu(a), false, false); return fmaxf(asf(r[0]), asf(r[1])); }
__device__ __forceinline__ float rowsum(float x) { x += dpp<X1>(x); x += dpp<X2>(x); x += dpp<HM>(x); x += dpp<R8>(x); return x; }
__device__ __forceinline__ float wavesum(float x) { x = rowsum(x); x = sw16(x, x); return sw32(x, x); }
__device__ __forceinline__ u32 umax(u32 a, u32 b) { return a > b ? a : b; }
__device__ __forceinline__ u32 max4(v4u v) { return umax(umax(v.x, v.y), umax(v.z, v.w)); }
__device__ __forceinline__ bool none(bool c) { return __builtin_amdgcn_ballot_w64(c) == 0; }
__device__ __forceinline__ u32 miss(v4u v, u32 t, u32 bad) { return ((v.y ^ t) | (v.w ^ t)) | bad; }
__device__ __forceinline__ v4u join(v4u v) { return v4u{v.x, dppu<R8>(v.x), v.z, dppu<R8>(v.z)}; }
__device__ __forceinline__ int perm4(int i) { return (i & ~3) | ((i & 1) << 1) | ((i >> 1) & 1); }
#define FENCE() asm volatile("" ::: "memory")
#define SB() __builtin_amdgcn_sched_barrier(0)
#define LOOP _Pragma("unroll 1") for (int it = 0; it < SPIN; ++it)
template <int N> __device__ __forceinline__ void nap() {
#pragma unroll
  for (int i = 0; i < N / 100; ++i) __builtin_amdgcn_s_sleep(100);
  if (N % 100) __builtin_amdgcn_s_sleep(N % 100);
}
__device__ __forceinline__ void lds_wait(LAS u32* f, u32 want) {
  _Pragma("unroll 1") for (int i = 0; i < (SPIN << 3); ++i) {
    if (__builtin_amdgcn_readfirstlane(__hip_atomic_load(f, __ATOMIC_RELAXED, __HIP_MEMORY_SCOPE_WORKGROUP)) >= want) break;
    __builtin_amdgcn_s_sleep(1);
  }
  FENCE();
}
__device__ __forceinline__ void lds_set(LAS u32* f) { FENCE(); __hip_atomic_store(f, 1u, __ATOMIC_RELAXED, __HIP_MEMORY_SCOPE_WORKGROUP); FENCE(); }
__device__ __forceinline__ u32 lds_arrive(LAS u32* f, int l) {
  FENCE();
  u32 old = 0;
  if (l == 0) old = __hip_atomic_fetch_add(f, 1u, __ATOMIC_RELAXED, __HIP_MEMORY_SCOPE_WORKGROUP);
  old = __builtin_amdgcn_readfirstlane(old);
  FENCE();
  return old;
}
__device__ __forceinline__ void bar() { asm volatile("s_waitcnt lgkmcnt(0)\n\ts_barrier" ::: "memory"); }
__device__ __forceinline__ void ln_consts(float s1, float s2, float& mb, float& rinv) {
  const float mean = s1 * (1.f / 640.f);
  const float var = fmaxf(fmaf(s2, 1.f / 640.f, -mean * mean), 0.f);
  mb = rbf(mean);
  rinv = __builtin_amdgcn_rcpf(rbf(__builtin_amdgcn_sqrtf(rbf(rbf(var) + 1e-5f))));
}
__device__ __forceinline__ void dma(rsrc_t r, int voff, int soff, int lds) {
  asm volatile("s_mov_b32 m0, %3\n\ts_nop 0\n\tbuffer_load_dwordx4 %0, %1, %2 offen" POL_W " lds" :: "v"(voff), "s"(r), "s"(soff), "s"(lds) : "memory");
}
__device__ __forceinline__ v4u frag(LAS char* p0, LAS char* p1) {
  const v2u lo = __builtin_bit_cast(v2u, __builtin_amdgcn_ds_read_tr16_b64_v4i16((LAS v4s*)p0));
  const v2u hi = __builtin_bit_cast(v2u, __builtin_amdgcn_ds_read_tr16_b64_v4i16((LAS v4s*)p1));
  return v4u{lo.x, lo.y, hi.x, hi.y};
}
__device__ __forceinline__ void axpy8(float (&acc)[8], v4u w, u32 s0, u32 s1) {
  acc[0] = dot2(w.x, s0, acc[0]); acc[1] = dot2(w.x, s1, acc[1]); acc[2] = dot2(w.y, s0, acc[2]); acc[3] = dot2(w.y, s1, acc[3]);
  acc[4] = dot2(w.z, s0, acc[4]); acc[5] = dot2(w.z, s1, acc[5]); acc[6] = dot2(w.w, s0, acc[6]); acc[7] = dot2(w.w, s1, acc[7]);
}
__device__ __forceinline__ v2f rows8(const float (&a)[8]) {
  const float t0 = sw32(a[0], a[4]), t1 = sw32(a[1], a[5]), t2 = sw32(a[2], a[6]), t3 = sw32(a[3], a[7]);
  return v2f{sw16(t0, t2), sw16(t1, t3)};
}
template <int N, int I = 0> __device__ __forceinline__ void bcast(u32 v, u32 (&o)[N]) {
  if constexpr (I < N) { o[I] = dppu<BC + I>(v); bcast<N, I + 1>(v, o); }
}

__global__ void __launch_bounds__(512, 2) fused_decode_kernel(Args a) {
  __shared__ __attribute__((aligned(16))) char smem[L_END];
  LAS char* L = (LAS char*)smem;
  LAS u32* F = (LAS u32*)smem;
  const CAS rsrc_t* kp = (const CAS rsrc_t*)__builtin_amdgcn_kernarg_segment_ptr();
  const int w = __builtin_amdgcn_readfirstlane(threadIdx.x >> 6);
  const int b = blockIdx.x, l = threadIdx.x & 63;
  u32 tsv = 0; (void)tsv;
  TS(0);
  const bool isQ = w < 4;
  const rsrc_t r0 = kp[isQ ? 3 : 1], rx = kp[0], rW = kp[8];
  SB();
  {
    u64 pc; u32 v;
    asm volatile("s_cmp_lt_u32 %2, 192\n\ts_cbranch_scc1 1f\n\t"
                 "s_getpc_b64 %0\n\ts_bitset0_b64 %0, 7\n\ts_bitset0_b64 %0, 6\n\t"
                 "v_lshlrev_b32 %1, 7, %3\n\tv_add_u32 %1, 0xffff4000, %1\n\tv_min_u32 %1, %4, %1\n\t"
                 "global_load_dword %1, %1, %0\n\ts_waitcnt vmcnt(0) lgkmcnt(0)\n\ts_endpgm\n1:"
                 : "=&s"(pc), "=&v"(v) : "s"(w < 6 ? 0 : b), "v"(threadIdx.x), "i"(CODE_BYTES - 128) : "scc", "memory");
  }
  SB();
  const int g = b & 7, j = b >> 3, kg = l >> 4, c = l & 15;
  const int gr = WS_GR + g * GR_SZ;
  const int hl = (j * 43) >> 8, s = j - 6 * hl, h = 4 * g + hl;
  const int kw = w & 3;
  const int uq0 = 27 * s - (s > 4 ? 1 : 0) + kw;
  const int sbq = uq0 * 32768 + h * 256, sbk = h * 1048576 + 786432 + (uq0 + 16 * s) * 1024;
  const int sb = isQ ? sbq : sbk, sh = isQ ? 13 : 8;
  int off0 = (kg << sh) + c * 16 + sb;
  const bool lastok = kw + (s > 3 ? 1 : 0) < 3;
  int offl = lastok ? off0 : OOB;
  const int xo1 = (4 * (uq0 + 4 * c) + kg) * 2, nx = lastok ? 7 : 6;
  int xo = c < nx ? xo1 : OOB;
  asm volatile("" : "+v"(off0), "+v"(offl), "+v"(xo) : "s"(r0), "s"(rx), "s"(rW));
  if (__builtin_expect(j < 24, 1)) {
    v4u xa, xb, wv[7], kq[11], vv[11]; u32 xs, ev;
    if (isQ) {
      xa = ld16<0>(rx, l * 16); xb = ld16<0>(rx, 1024 + l * 16);
      xs = __builtin_amdgcn_raw_buffer_load_b16(rx, xo, 0, 0);
      ev = __builtin_amdgcn_raw_buffer_load_b32(rW, WS_E, 0, SC1);
#pragma unroll
      for (int i = 0; i < 7; ++i) wv[i] = ld16<AUX_S>(r0, i == 6 ? offl : off0, i * 131072);
      SB();
      TS(15);
      __builtin_amdgcn_s_barrier();
      TS(9);
    }
    int role = w;
    asm volatile("" : "+s"(role));
    if (role >= 4) {
      const rsrc_t rV = kp[2];
      if (w == 4 && l < 16) F[l] = 0;
      TS(15);
      bar();
      if (DLY_K) nap<DLY_K>();
      TS(14);
#pragma unroll
      for (int i = 0; i < 11; ++i) kq[i] = ld16<AUX_S>(r0, i == 10 ? offl : off0, i * 4096);
      SB();
      TS(9);
#pragma unroll
      for (int i = 0; i < 11; ++i) vv[i] = ld16<AUX_S>(rV, i == 10 ? offl : off0, i * 4096);
      SB();
      TS(11);
    }
    asm volatile("" : "+s"(role));
    if (role < 4) {
      asm volatile("" : "+v"(xs), "+v"(ev));
      const u32 T = ev + 1;
      {
        const u32 one2 = 0x3F803F80u;
        float s1 = dot2(xa.x, one2, dot2(xa.y, one2, dot2(xa.z, one2, dot2(xa.w, one2, 0.f))));
        float s2 = dot2(xa.x, xa.x, dot2(xa.y, xa.y, dot2(xa.z, xa.z, dot2(xa.w, xa.w, 0.f))));
        s1 = dot2(xb.x, one2, dot2(xb.y, one2, dot2(xb.z, one2, dot2(xb.w, one2, s1))));
        s2 = dot2(xb.x, xb.x, dot2(xb.y, xb.y, dot2(xb.z, xb.z, dot2(xb.w, xb.w, s2))));
        s1 = wavesum(s1); s2 = wavesum(s2);
        float mb, rinv; ln_consts(s1, s2, mb, rinv);
        const u32 xn = pk(rbf(blo(xs) - mb) * rinv, 0.f) & 0xffffu;
        u32 x0[7], x1[7];
        bcast<7>(xn, x0); bcast<7>(xn << 16, x1);
        TS(10);
        float acc[8] = {0, 0, 0, 0, 0, 0, 0, 0};
#pragma unroll
        for (int i = 0; i < 7; ++i) { axpy8(acc, wv[i], x0[i], x1[i]); SB(); }
        ((LAS v2f*)(L + L_QP))[kw * 64 + 4 * c + kg] = rows8(acc);
      }
      TS(1);
      const bool qlast = lds_arrive(F + F_QCNT, l) == 3;
      if (qlast) {
        const v2f q = (((LAS v2f*)(L + L_QP))[l] + ((LAS v2f*)(L + L_QP))[64 + l]) + (((LAS v2f*)(L + L_QP))[128 + l] + ((LAS v2f*)(L + L_QP))[192 + l]);
        __builtin_amdgcn_raw_buffer_store_b128(v4u{asu(q.x), T, asu(q.y), T}, rW, WS_Q + (h * 6 + s) * 1024 + l * 16, 0, SC1);
        TS(2);
        nap<DLY_Q>();
        float q0, q1;
        LOOP {
          FENCE();
          v4u v[6];
#pragma unroll
          for (int p = 0; p < 6; ++p) v[p] = ld16<SC1>(rW, WS_Q + h * 6144 + l * 16, p * 1024);
          u32 bad = 0;
          q0 = 0.f; q1 = 0.f;
#pragma unroll
          for (int p = 0; p < 6; ++p) { bad = miss(v[p], T, bad); q0 += asf(v[p].x); q1 += asf(v[p].z); }
          if (none(bad != 0)) break;
        }
        ((LAS u32*)(L + L_Q))[l] = pk(q0, q1);
        F[F_TAG] = T;
        lds_set(F + F_QRDY);
        TS(3);
      }
      if (s == 5) RET();
      const int cb = hl * 5 + s;
      lds_wait(F + F_QRDY, 1);
      const rsrc_t rO = kp[4];
      const int vo = ((g * 4 + w) * 128 + (l >> 2)) * 1280 + cb * 64 + ((l & 3) ^ ((l >> 4) & 2)) * 16;
      if (WO_EARLY) {
        lds_wait(F + F_VCNT, 4);
#pragma unroll
        for (int i = 0; i < WO_EARLY; ++i) dma(rO, vo, i * 20480, L_IMG + w * 8192 + i * 1024);
      }
      const bool aok = (c & 7) < 6;
      const int aoff = aok ? gr + GR_A + (w * 6 + (c & 7)) * 640 + kg * 32 + (c >> 3) * 16 : OOB;
      const int mp = (c + 4 * kg) & 7;
      const int moff = mp < 6 ? gr + GR_A + (w * 6 + mp) * 640 + 512 : OOB;
      const u32 Ta = aok ? T : 0u, Tm = mp < 6 ? T : 0u;
      lds_wait(F + F_APUB, 1);
#pragma unroll
      for (int i = WO_EARLY; i < 8; ++i) dma(rO, vo, i * 20480, L_IMG + w * 8192 + i * 1024);
      nap<DLY_A>();
      TS(4);
      v4u av[4], ml;
#pragma unroll
      for (int t = 0; t < 4; ++t) av[t] = ld16<SC1>(rW, aoff + t * 128);
      ml = ld16<SC1>(rW, moff);
      SB();
      asm volatile("s_waitcnt vmcnt(5)" ::: "memory");
      v4u bf[4][2];
      {
        LAS char* p = L + L_IMG + w * 8192 + (8 * kg + (c >> 2)) * 64 + (c & 3) * 8;
        LAS char* p0 = p + (kg & 1) * 32; LAS char* p1 = p + ((kg & 1) ^ 1) * 32;
#pragma unroll
        for (int t = 0; t < 4; ++t) { bf[t][0] = frag(p0 + t * 2048, p0 + t * 2048 + 256); bf[t][1] = frag(p1 + t * 2048, p1 + t * 2048 + 256); }
      }
      SB();
      TS(11);
      LOOP {
        if (none(miss(av[0], Ta, miss(av[1], Ta, miss(av[2], Ta, miss(av[3], Ta, miss(ml, Tm, 0u))))) != 0)) break;
        FENCE();
#pragma unroll
        for (int t = 0; t < 4; ++t) av[t] = ld16<SC1>(rW, aoff + t * 128);
        ml = ld16<SC1>(rW, moff);
      }
      TS(5);
      {
        v4f y0 = {0.f, 0.f, 0.f, 0.f}, y1 = y0;
#pragma unroll
        for (int t = 0; t < 4; ++t) { const v4u af = join(av[t]); y0 = mfma(af, bf[t][0], y0); y1 = mfma(af, bf[t][1], y1); }
        const float m = mp < 6 ? asf(ml.x) : -INFINITY;
        float M = fmaxf(m, dpp<X1>(m)); M = fmaxf(M, dpp<X2>(M)); M = fmaxf(M, dpp<HM>(M));
        const float al = __builtin_amdgcn_exp2f(m - M);
        float Ls = al * asf(ml.z); Ls += dpp<X1>(Ls); Ls += dpp<X2>(Ls); Ls += dpp<HM>(Ls);
        const float fp = al * __builtin_amdgcn_rcpf(Ls);
        const float f0 = dpp<BC>(fp), f1 = dpp<BC + 1>(fp), f2 = dpp<BC + 2>(fp), f3 = dpp<BC + 3>(fp);
        const float z0 = fmaf(f0, y0.x, f1 * y0.y) + fmaf(f2, y0.z, f3 * y0.w), z1 = fmaf(f0, y1.x, f1 * y1.y) + fmaf(f2, y1.z, f3 * y1.w);
        const float z = sw16(z0, z1);
        if (l < 32) ((LAS float*)(L + L_YP))[w * 32 + l] = z;
      }
      if (lds_arrive(F + F_YCNT, l) != 3) RET();
      {
        LAS v2f* yp = (LAS v2f*)(L + L_YP) + 4 * ((l >> 1) & 3) + (l & 1);
        const v2f t0 = (yp[0] + yp[16]) + (yp[32] + yp[48]), t1 = (yp[2] + yp[18]) + (yp[34] + yp[50]);
        __builtin_amdgcn_raw_buffer_store_b128(v4u{pk(t0.x, t0.y), T, pk(t1.x, t1.y), T}, rW,
            WS_GR + (l >> 3) * GR_SZ + GR_B + (g * 20 + cb) * 128 + (l & 7) * 16, 0, SC1);
      }
      TS(6);
      if (hl != 0) RET();
      const int r = g * 5 + s, col = r * 16 + 2 * (l & 7) + (l >> 5);
      const u32 xr = __builtin_amdgcn_raw_buffer_load_b16(rx, col * 2, 0, 0);
      float hv;
      {
        const int ho = gr + GR_B + (((l >> 3) & 3) * 20 + (r >> 1)) * 128 + perm4(8 * (r & 1) + (l & 7)) * 8;
        v2u d0, d1;
        LOOP {
          FENCE();
          d0 = __builtin_amdgcn_raw_buffer_load_b64(rW, ho, 0, SC1);
          d1 = __builtin_amdgcn_raw_buffer_load_b64(rW, ho, 80 * 128, SC1);
          if (none(((d0.y ^ T) | (d1.y ^ T)) != 0)) break;
        }
        float t = l >= 32 ? bhi(d0.x) + bhi(d1.x) : blo(d0.x) + blo(d1.x);
        t += dpp<R8>(t); t = sw16(t, t);
        hv = rbf(blo(xr) + rbf(t));
      }
      v4u dv[7];
      const int doff = gr + GR_D + s * 0x2000 + l * 16, doff6 = l < 48 ? doff : OOB;
      const u32 T6 = l < 48 ? T : 0u;
      const rsrc_t rOut = kp[7];
      const int ooff = (l & 24) == 0 ? col * 2 : OOB;
      LOOP {
        FENCE();
#pragma unroll
        for (int i = 0; i < 7; ++i) dv[i] = ld16<SC1>(rW, i == 6 ? doff6 : doff, i * 1024);
        u32 bad = 0;
#pragma unroll
        for (int i = 0; i < 7; ++i) bad = miss(dv[i], i == 6 ? T6 : T, bad);
        if (none(bad != 0)) break;
      }
      TS(7);
      {
        const float a0 = ((asf(dv[0].x) + asf(dv[1].x)) + (asf(dv[2].x) + asf(dv[3].x))) + ((asf(dv[4].x) + asf(dv[5].x)) + asf(dv[6].x));
        const float a1 = ((asf(dv[0].z) + asf(dv[1].z)) + (asf(dv[2].z) + asf(dv[3].z))) + ((asf(dv[4].z) + asf(dv[5].z)) + asf(dv[6].z));
        float z = sw32(a0, a1);
        z += dpp<R8>(z); z = sw16(z, z);
        __builtin_amdgcn_raw_buffer_store_b16((unsigned short)pk(hv + rbf(z), 0.f), rOut, ooff, 0, 0);
        if (b == 0) __builtin_amdgcn_raw_buffer_store_b32(T, rW, l == 0 ? WS_E : OOB, 0, SC1);
      }
      TS(8);
    } else {
      lds_arrive(F + F_VCNT, l);
      TS(1);
      v4u fa[8]; v2u fb[8];
#pragma unroll
      for (int e = 0; e < 8; ++e) {
        const u32 sel = (e & 1) ? 0x07060302u : 0x05040100u;
#pragma unroll
        for (int i = 0; i < 4; ++i) if (2 * i + 1 < 11 - V_TAIL) fa[e][i] = __builtin_amdgcn_perm(vv[2 * i + 1][e >> 1], vv[2 * i][e >> 1], sel);
      }
#pragma unroll
      for (int e = 0; e < 8; ++e)
#pragma unroll
        for (int i = 0; i < 4; ++i) if (2 * i + 1 < 11 - V_TAIL) asm volatile("" : "+v"(fa[e][i]));
      if (V_TAIL == 0) {
#pragma unroll
        for (int e = 0; e < 8; ++e) {
          fb[e][0] = __builtin_amdgcn_perm(vv[9][e >> 1], vv[8][e >> 1], (e & 1) ? 0x07060302u : 0x05040100u);
          fb[e][1] = (e & 1) ? vv[10][e >> 1] >> 16 : vv[10][e >> 1] & 0xffffu;
          asm volatile("" : "+v"(fb[e]));
        }
      }
      SB();
      TS(13);
      lds_wait(F + F_QRDY, 1);
      TS(12);
      const v4u q = ((LAS v4u*)(L + L_Q))[c];
      float d[11];
#pragma unroll
      for (int i = 0; i < 11; ++i) d[i] = dot2(kq[i].w, q.w, dot2(kq[i].z, q.z, dot2(kq[i].y, q.y, dot2(kq[i].x, q.x, 0.f))));
#pragma unroll
      for (int i = 0; i < 8; ++i) { const float lo = d[i] + dpp<R8>(d[i]); if (i < 3) { const float hi = d[i + 8] + dpp<R8>(d[i + 8]); d[i] = (c & 8) ? hi : lo; } else d[i] = lo; }
#pragma unroll
      for (int i = 0; i < 4; ++i) { const float lo = d[i] + dpp<HM>(d[i]), hi = d[i + 4] + dpp<HM>(d[i + 4]); d[i] = (c & 4) ? hi : lo; }
#pragma unroll
      for (int i = 0; i < 2; ++i) { const float lo = d[i] + dpp<X2>(d[i]), hi = d[i + 2] + dpp<X2>(d[i + 2]); d[i] = (c & 2) ? hi : lo; }
      float sc;
      { const float lo = d[0] + dpp<X1>(d[0]), hi = d[1] + dpp<X1>(d[1]); sc = (c & 1) ? hi : lo; }
      sc = c < nx + 4 ? rbf(rbf(sc) * 0.08838834764831845f) * 1.4426950408889634f : -INFINITY;
      float m = fmaxf(sc, dpp<X1>(sc)); m = fmaxf(m, dpp<X2>(m)); m = fmaxf(m, dpp<HM>(m)); m = fmaxf(m, dpp<R8>(m));
      m = mx32(mx16(m));
      const u32 pb = pk(__builtin_amdgcn_exp2f(sc - m), 0.f) & 0xffffu;
      const u32 pp = pb | (dppu<X1>(pb) << 16);
      const v4u pa = {dppu<BC>(pp), dppu<BC + 2>(pp), dppu<BC + 4>(pp), dppu<BC + 6>(pp)};
      const v2u pc2 = {dppu<BC + 8>(pp), dppu<BC + 10>(pp)};
      TS(2);
      SB();
#pragma unroll
      for (int e = 0; e < 8; ++e) {
        const u32 sel = (e & 1) ? 0x07060302u : 0x05040100u;
#pragma unroll
        for (int i = 0; i < 4; ++i) if (2 * i + 1 >= 11 - V_TAIL) fa[e][i] = __builtin_amdgcn_perm(vv[2 * i + 1][e >> 1], vv[2 * i][e >> 1], sel);
      }
      v4f acc[8];
#pragma unroll
      for (int e = 0; e < 8; ++e) acc[e] = mfma(pa, fa[e], v4f{0.f, 0.f, 0.f, 0.f});
      const float ls = wavesum(blo(pb));
      if (l == 0) { ((LAS float*)(L + L_M))[kw] = m; ((LAS float*)(L + L_L))[kw] = ls; }
#pragma unroll
      for (int e = 0; e < 8; ++e) {
        if (V_TAIL != 0) {
          fb[e][0] = __builtin_amdgcn_perm(vv[9][e >> 1], vv[8][e >> 1], (e & 1) ? 0x07060302u : 0x05040100u);
          fb[e][1] = (e & 1) ? vv[10][e >> 1] >> 16 : vv[10][e >> 1] & 0xffffu;
        }
        acc[e] = __builtin_amdgcn_mfma_f32_16x16x16bf16_1k(__builtin_bit_cast(v4s, pc2), __builtin_bit_cast(v4s, fb[e]), acc[e], 0, 0, 0);
      }
      TS(3);
      {
        const float o0 = (kg & 2) ? ((kg & 1) ? acc[6].x : acc[4].x) : ((kg & 1) ? acc[2].x : acc[0].x);
        const float o1 = (kg & 2) ? ((kg & 1) ? acc[7].x : acc[5].x) : ((kg & 1) ? acc[3].x : acc[1].x);
        ((LAS v2f*)(L + L_OP))[kw * 64 + 4 * c + kg] = v2f{o0, o1};
      }
      if (lds_arrive(F + F_OCNT, l) != 3) RET();
      const v4f m4 = *(LAS v4f*)(L + L_M), l4 = *(LAS v4f*)(L + L_L);
      LAS v2f* op = (LAS v2f*)(L + L_OP) + perm4(l);
      const v2f o_0 = op[0], o_1 = op[64], o_2 = op[128], o_3 = op[192];
      const u32 T = F[F_TAG];
      const float M = fmaxf(fmaxf(m4.x, m4.y), fmaxf(m4.z, m4.w));
      const float f0 = __builtin_amdgcn_exp2f(m4.x - M), f1 = __builtin_amdgcn_exp2f(m4.y - M), f2 = __builtin_amdgcn_exp2f(m4.z - M), f3 = __builtin_amdgcn_exp2f(m4.w - M);
      const v2f o = (o_0 * f0 + o_1 * f1) + (o_2 * f2 + o_3 * f3);
      __builtin_amdgcn_raw_buffer_store_b64(v2u{pk(o.x, o.y), T}, rW, gr + GR_A + j * 640 + l * 8, 0, SC1);
      __builtin_amdgcn_raw_buffer_store_b128(v4u{asu(M), T, asu(fmaf(f0, l4.x, f1 * l4.y) + fmaf(f2, l4.z, f3 * l4.w)), T}, rW,
                                             l < 8 ? gr + GR_A + j * 640 + 512 + l * 16 : OOB, 0, SC1);
      lds_set(F + F_APUB);
      TS(4);
    }
  } else {
    const int jj = j - 24;
    if (jj >= (g < 3 ? 8 : 6) || w >= 4) RET();
    const int m = 2 * (g < 3 ? 4 * g : 3 * g + 3) + jj;
    u32 T = __builtin_amdgcn_raw_buffer_load_b32(rW, WS_E, 0, SC1);
    v4u cf[5];
#pragma unroll
    for (int t = 0; t < 5; ++t) cf[t] = ld16<0>(rx, c == 8 ? (160 * w + 8 * kg) * 2 : OOB, t * 64);
    nap<DLY_M>();
    {
      const rsrc_t r1 = kp[5], r2 = kp[6];
      const int v1 = (160 * w + (l >> 2)) * 3456 + m * 64 + ((l & 3) ^ ((l >> 4) & 2)) * 16;
#pragma unroll
      for (int i = 0; i < 10; ++i) dma(r1, v1, i * 16 * 3456, L_IMG + w * 10240 + i * 1024);
#pragma unroll
      for (int i = 0; i < 10; ++i) dma(r2, l * 16, m * 40960 + (w * 10 + i) * 1024, L_IMG + 40960 + (w * 10 + i) * 1024);
    }
    asm volatile("s_waitcnt vmcnt(0)" ::: "memory");
    TS(6);
    bar();
    v4u b1[5][2], b2[10];
    {
      LAS char* p = L + L_IMG + w * 10240 + (8 * kg + (c >> 2)) * 64 + (c & 3) * 8;
      LAS char* p0 = p + (kg & 1) * 32; LAS char* p1 = p + ((kg & 1) ^ 1) * 32;
#pragma unroll
      for (int t = 0; t < 5; ++t) { b1[t][0] = frag(p0 + t * 2048, p0 + t * 2048 + 256); b1[t][1] = frag(p1 + t * 2048, p1 + t * 2048 + 256); }
      LAS char* p2 = L + L_IMG + 40960 + w * 320 + (8 * kg + (c >> 2)) * 1280 + (c & 3) * 8;
#pragma unroll
      for (int t = 0; t < 10; ++t) b2[t] = frag(p2 + t * 32, p2 + t * 32 + 5120);
    }
    {
      v4f k0 = {0.f, 0.f, 0.f, 0.f}, k1 = k0, kx = k0;
      const u32 one = c == 9 ? 0x3F803F80u : 0u;
#pragma unroll
      for (int t = 0; t < 5; ++t) {
        cf[t] |= one;
        k0 = mfma(cf[t], b1[t][0], k0); k1 = mfma(cf[t], b1[t][1], k1); kx = mfma(cf[t], cf[t], kx);
      }
      if (kg == 2) {
        ((LAS float*)(L + L_XW))[w * 32 + c] = k0.x; ((LAS float*)(L + L_XW))[w * 32 + 16 + c] = k1.x;
        ((LAS float*)(L + L_CS))[w * 32 + c] = k0.y; ((LAS float*)(L + L_CS))[w * 32 + 16 + c] = k1.y;
        if (c == 8 || c == 9) ((LAS float*)(L + L_SX))[w * 2 + 9 - c] = kx.x;
      }
    }
    TS(1);
    bar();
    TS(2);
    const int pi = 4 * kg + (c & 3);
    v2f xw, cs, sx;
    {
      LAS v2f* px = (LAS v2f*)(L + L_XW) + pi; LAS v2f* pc2 = (LAS v2f*)(L + L_CS) + pi; LAS v2f* ps = (LAS v2f*)(L + L_SX);
      xw = (px[0] + px[16]) + (px[32] + px[48]); cs = (pc2[0] + pc2[16]) + (pc2[32] + pc2[48]); sx = (ps[0] + ps[1]) + (ps[2] + ps[3]);
    }
    const float wg = c < 8 ? 1.f : c == 8 ? 2.f : 0.f;
    const int boff = gr + GR_B + ((c & 7) * 20 + 5 * w) * 128 + kg * 32 + (c >> 3) * 16;
    T += 1;
    int so[3];
#pragma unroll
    for (int i = 0; i < 3; ++i) {
      const int r = 10 * w + 4 * i + kg, rg = r / 5;
      so[i] = 4 * i + kg < 10 ? WS_GR + rg * GR_SZ + GR_D + (r - 5 * rg) * 0x2000 + m * 128 + c * 8 : OOB;
      asm volatile("" : "+v"(so[i]));
    }
    asm volatile("" : "+v"(xw), "+v"(cs), "+v"(sx), "+v"(T));
    v4u av[5];
    LOOP {
      FENCE();
#pragma unroll
      for (int t = 0; t < 5; ++t) av[t] = ld16<SC1>(rW, boff + t * 128);
      if (none(miss(av[0], T, miss(av[1], T, miss(av[2], T, miss(av[3], T, miss(av[4], T, 0u))))) != 0)) break;
    }
    TS(3);
    {
      v4f y0 = {0.f, 0.f, 0.f, 0.f}, y1 = y0, gm = y0;
#pragma unroll
      for (int t = 0; t < 5; ++t) { av[t] = join(av[t]); gm = mfma(av[t], c < 8 ? av[t] : cf[t], gm); }
#pragma unroll
      for (int t = 0; t < 5; ++t) { y0 = mfma(av[t], b1[t][0], y0); y1 = mfma(av[t], b1[t][1], y1); }
      const float tg = (gm.x + gm.y) + (gm.z + gm.w);
      const float tt = sw16(tg * wg, tg);
      const float ts = rowsum(tt);
      if (l == 0 || l == 25) ((LAS float*)(L + L_ST))[w * 2 + (l == 0)] = l == 0 ? ts : tt;
      const float z = sw16((y0.x + y0.y) + (y0.z + y0.w), (y1.x + y1.y) + (y1.z + y1.w));
      if (l < 32) ((LAS float*)(L + L_P1))[w * 32 + l] = z;
    }
    bar();
    TS(4);
    {
      LAS v2f* pp = (LAS v2f*)(L + L_P1) + pi;
      const v4f s0 = *(LAS v4f*)(L + L_ST), s1 = *(LAS v4f*)(L + L_ST + 16);
      const v2f hw = ((pp[0] + pp[16]) + (pp[32] + pp[48])) + xw;
      const v4f st = s0 + s1;
      float mb, rinv; ln_consts((st.x + st.z) + sx.x, (st.y + st.w) + sx.y, mb, rinv);
      const float u0 = fmaf(-mb, cs.x, hw.x) * rinv, u1 = fmaf(-mb, cs.y, hw.y) * rinv;
      const u32 hp = pk(u0 < 0.f ? 0.f : u0, u1 < 0.f ? 0.f : u1);
      const v4u ha = {dppu<0x00>(hp), dppu<0x55>(hp), dppu<0xAA>(hp), dppu<0xFF>(hp)};
      float o[12];
      const v4f z = {0.f, 0.f, 0.f, 0.f};
#pragma unroll
      for (int t = 0; t < 10; ++t) o[t] = mfma(ha, b2[t], z).x;
      o[10] = o[8]; o[11] = o[9];
#pragma unroll
      for (int i = 0; i < 3; ++i) {
        const float v = (kg & 2) ? ((kg & 1) ? o[4 * i + 3] : o[4 * i + 2]) : ((kg & 1) ? o[4 * i + 1] : o[4 * i]);
        __builtin_amdgcn_raw_buffer_store_b64(v2u{asu(v), T}, rW, so[i], 0, SC1);
      }
    }
    TS(5);
  }
  RET();
}

extern "C" void kernel_launch(void* const* d_in, const int* in_sizes, int n_in, void* d_out, int out_size, void* d_ws, size_t ws_size,
                              hipStream_t stream) {
  Args a;
  const int idx[7] = {0, 1, 2, 3, 4, 6, 7};
  for (int i = 0; i < 7; ++i) a.t[i] = Srd{(const char*)d_in[idx[i]], (u32)in_sizes[idx[i]] * 2u, 0x00020000u};
  a.t[7] = Srd{(const char*)d_out, (u32)out_size * 2u, 0x00020000u};
  a.t[8] = Srd{(const char*)d_ws, (u32)WS_BYTES, 0x00020000u};
  fused_decode_kernel<<<256, 512, 0, stream>>>(a);
}
```
